```python
import torch

def _softmax(x, axis=-1):
    return torch.softmax(x, dim=axis)
def _gelu(x):
    return torch.nn.functional.gelu(x, approximate="tanh")
def _silu(x):
    return torch.nn.functional.silu(x)
def _sigmoid(x):
    return torch.sigmoid(x)
def _layernorm(x, axis=-1, eps=1e-5):
    m = x.mean(dim=axis, keepdim=True)
    v = x.var(dim=axis, keepdim=True, unbiased=False)
    return (x - m) / torch.sqrt(v + eps)
def _scatter_add(tgt, idx, src, axis=0):
    return tgt.clone().index_add_(axis, idx, src)
def _segment_sum(src, seg, n):
    out = torch.zeros((n,) + src.shape[1:], dtype=src.dtype)
    return out.index_add_(0, seg, src)
def _pad(x, pw):
    flat = tuple(v for pair in reversed(pw) for v in pair)
    return torch.nn.functional.pad(x, flat)

kernel_name = 'concat_heads__gqa_block_cached__gqa_block_cached__near_ridge'

def setup_inputs():

    _w = (lambda a: (lambda t: t.to(torch.bfloat16) if t.is_floating_point() else t)(torch.as_tensor(a)))
    def _A_setup():
        B, Sq, D = 8, 96, 640
        Hq, Hkv, Sk, Dh = 32, 32, 96, 128
        assert Hq % Hkv == 0, (Hq, Hkv)
        return {
            "x": _w((lambda *s: torch.randn(*s, dtype=torch.bfloat16) * 0.8)(B, Sq, D)),
            "k_cache": _w((lambda *s: torch.randn(*s, dtype=torch.bfloat16) * 0.8)(B, Hkv, Sk, Dh)),
            "v_cache": _w((lambda *s: torch.randn(*s, dtype=torch.bfloat16) * 0.05)(B, Hkv, Sk, Dh)),
            "wq": _w((lambda *s: torch.randn(*s, dtype=torch.bfloat16) * 0.8)(D, Hq * Dh) * (D ** -0.5)),
            "wo": _w((lambda *s: torch.randn(*s, dtype=torch.bfloat16) * 0.05)(Hq * Dh, D) * ((Hq * Dh) ** -0.5)),
        }
    _A_in = _A_setup()

    def _B_setup():
        B, Sq, D = 8, 96, 640
        Hq, Hkv, Sk, Dh = 32, 32, 96, 128
        assert Hq % Hkv == 0, (Hq, Hkv)
        return {
            "x": _w((lambda *s: torch.randn(*s, dtype=torch.bfloat16) * 0.8)(B, Sq, D)),
            "k_cache": _w((lambda *s: torch.randn(*s, dtype=torch.bfloat16) * 0.8)(B, Hkv, Sk, Dh)),
            "v_cache": _w((lambda *s: torch.randn(*s, dtype=torch.bfloat16) * 0.05)(B, Hkv, Sk, Dh)),
            "wq": _w((lambda *s: torch.randn(*s, dtype=torch.bfloat16) * 0.8)(D, Hq * Dh) * (D ** -0.5)),
            "wo": _w((lambda *s: torch.randn(*s, dtype=torch.bfloat16) * 0.05)(Hq * Dh, D) * ((Hq * Dh) ** -0.5)),
        }
    _B_in = _B_setup()

    return {
        "A_x": _A_in["x"],
        "A_k_cache": _A_in["k_cache"],
        "A_v_cache": _A_in["v_cache"],
        "A_wq": _A_in["wq"],
        "A_wo": _A_in["wo"],
        "B_x": _B_in["x"],
        "B_k_cache": _B_in["k_cache"],
        "B_v_cache": _B_in["v_cache"],
        "B_wq": _B_in["wq"],
        "B_wo": _B_in["wo"],
    }

def reference(A_x, A_k_cache, A_v_cache, A_wq, A_wo, B_x, B_k_cache, B_v_cache, B_wq, B_wo):
    def _A_ref(x, k_cache, v_cache, wq, wo):
        B, Sq, D = x.shape
        Hq, Dh = 32, 128
        Hkv = k_cache.shape[1]
        rep = Hq // Hkv
        q = (lambda x, *d: x.permute(*d))((x @ wq).reshape(B, Sq, Hq, Dh), 0, 2, 1, 3)
        k = (lambda x, n, axis: torch.repeat_interleave(x, n, dim=axis))(k_cache, rep, axis=1)
        v = (lambda x, n, axis: torch.repeat_interleave(x, n, dim=axis))(v_cache, rep, axis=1)
        scores = torch.einsum("bhqd,bhkd->bhqk", q, k) * (Dh ** -0.5)
        p = _softmax(scores, axis=-1)
        o = torch.einsum("bhqk,bhkd->bhqd", p, v)
        return (lambda x, *d: x.permute(*d))(o, 0, 2, 1, 3).reshape(B, Sq, Hq * Dh) @ wo

    def _B_ref(x, k_cache, v_cache, wq, wo):
        B, Sq, D = x.shape
        Hq, Dh = 32, 128
        Hkv = k_cache.shape[1]
        rep = Hq // Hkv
        q = (lambda x, *d: x.permute(*d))((x @ wq).reshape(B, Sq, Hq, Dh), 0, 2, 1, 3)
        k = (lambda x, n, axis: torch.repeat_interleave(x, n, dim=axis))(k_cache, rep, axis=1)
        v = (lambda x, n, axis: torch.repeat_interleave(x, n, dim=axis))(v_cache, rep, axis=1)
        scores = torch.einsum("bhqd,bhkd->bhqk", q, k) * (Dh ** -0.5)
        p = _softmax(scores, axis=-1)
        o = torch.einsum("bhqk,bhkd->bhqd", p, v)
        return (lambda x, *d: x.permute(*d))(o, 0, 2, 1, 3).reshape(B, Sq, Hq * Dh) @ wo

    a = _A_ref(A_x, A_k_cache, A_v_cache, A_wq, A_wo)
    b = _B_ref(B_x, B_k_cache, B_v_cache, B_wq, B_wo)
    return torch.concatenate([a, b], axis=-1)
```

```cpp
#include <hip/hip_runtime.h>
#include <hip/hip_bf16.h>
typedef unsigned u32x4 __attribute__((ext_vector_type(4)));
typedef unsigned u32x2 __attribute__((ext_vector_type(2)));
typedef short s16x4 __attribute__((ext_vector_type(4)));
typedef __bf16 bf16x8 __attribute__((ext_vector_type(8)));
typedef __bf16 bf16x2 __attribute__((ext_vector_type(2)));
typedef float f32x4 __attribute__((ext_vector_type(4)));
typedef float f32x2 __attribute__((ext_vector_type(2)));
#define DI __device__ __forceinline__
#define LAS __attribute__((address_space(3)))
DI f32x4 mfma(u32x4 a, u32x4 b, f32x4 c) { return __builtin_amdgcn_mfma_f32_16x16x32_bf16(__builtin_bit_cast(bf16x8, a), __builtin_bit_cast(bf16x8, b), c, 0, 0, 0); }
DI unsigned cvtpk(float lo, float hi) { f32x2 v = {lo, hi}; bf16x2 b = __builtin_convertvector(v, bf16x2); return __builtin_bit_cast(unsigned, b); }
DI u32x2 trread(LAS char* p) { return __builtin_bit_cast(u32x2, __builtin_amdgcn_ds_read_tr16_b64_v4i16((LAS s16x4*)p)); }
DI u32x4 cat(u32x2 a, u32x2 b) { u32x4 r = {a[0], a[1], b[0], b[1]}; return r; }
DI u32x4 pack2(f32x4 a, f32x4 b) { u32x4 r = {cvtpk(a[0], a[1]), cvtpk(a[2], a[3]), cvtpk(b[0], b[1]), cvtpk(b[2], b[3])}; return r; }

struct Args { const char* x[2]; const char* k[2]; const char* v[2]; const char* wq[2]; const char* wo[2]; char* ows; char* out; };

__global__ __launch_bounds__(256, 1) void p1(Args a) {
  extern __shared__ __attribute__((aligned(16))) char smem[];
  LAS char* lds = (LAS char*)smem;
  const unsigned tid = threadIdx.x, lane = tid & 63, w = __builtin_amdgcn_readfirstlane(tid >> 6);
  const unsigned xcd = blockIdx.x & 7, idx = blockIdx.x >> 3;
  const unsigned br = xcd >> 2, hg = xcd & 3, h = hg * 8 + (idx & 7), bp = idx >> 3;
  const unsigned c = lane & 15, g = lane >> 4, qq = c >> 2, p = lane & 3;
  const char* X = a.x[br] + (size_t)bp * 192 * 1280;
  const char* W = a.wq[br] + h * 256;
  LAS char* lw = lds;
  LAS char* lx = lds + 16384;
  LAS char* lk = lds + 40960;
  LAS char* lv = lds + 40960 + 49152;
  for (int i = 0; i < 12; ++i) {
    unsigned n = tid + 256 * i, bb = n / 1536, r = (n % 1536) >> 4, ch = n & 15;
    size_t go = ((size_t)(bp * 2 + bb) * 32 + h) * 24576 + r * 256 + ch * 16;
    *(LAS u32x4*)(lk + bb * 24576 + r * 256 + ((ch ^ (r & 15)) << 4)) = *(const u32x4*)(a.k[br] + go);
    *(LAS u32x4*)(lv + bb * 24576 + r * 256 + ((ch ^ ((r & 7) << 1)) << 4)) = *(const u32x4*)(a.v[br] + go);
  }
  f32x4 acc[8][3];
  #pragma unroll
  for (int i = 0; i < 8; ++i)
    #pragma unroll
    for (int t = 0; t < 3; ++t) acc[i][t] = f32x4{0, 0, 0, 0};
  const unsigned fk = qq | ((g & 1) << 2);
  for (int ck = 0; ck < 10; ++ck) {
    u32x4 rw[4], rx[6];
    #pragma unroll
    for (int i = 0; i < 4; ++i) { unsigned n = tid + 256 * i, r = n >> 4, ch = n & 15; rw[i] = *(const u32x4*)(W + (size_t)(ck * 64 + r) * 8192 + ch * 16); }
    #pragma unroll
    for (int i = 0; i < 6; ++i) { unsigned n = tid + 256 * i, r = n >> 3, ch = n & 7; rx[i] = *(const u32x4*)(X + (size_t)r * 1280 + ck * 128 + ch * 16); }
    __syncthreads();
    #pragma unroll
    for (int i = 0; i < 4; ++i) { unsigned n = tid + 256 * i, r = n >> 4, ch = n & 15; unsigned f = (r & 3) | ((r >> 1) & 4); *(LAS u32x4*)(lw + r * 256 + ((ch ^ (f << 1)) << 4)) = rw[i]; }
    #pragma unroll
    for (int i = 0; i < 6; ++i) { unsigned n = tid + 256 * i, r = n >> 3, ch = n & 7; *(LAS u32x4*)(lx + r * 128 + ((ch ^ ((r >> 1) & 7)) << 4)) = rx[i]; }
    __syncthreads();
    #pragma unroll
    for (int s = 0; s < 2; ++s) {
      u32x4 bf[3];
      #pragma unroll
      for (int t = 0; t < 3; ++t) { unsigned r = w * 48 + 16 * t + c; bf[t] = *(LAS u32x4*)(lx + r * 128 + (((4 * s + g) ^ ((r >> 1) & 7)) << 4)); }
      #pragma unroll
      for (int i = 0; i < 8; ++i) {
        LAS char* base = lw + (32 * s + 8 * g + qq) * 256 + (((2 * i + (p >> 1)) ^ (fk << 1)) << 4) + 8 * (p & 1);
        u32x4 af = cat(trread(base), trread(base + 4 * 256));
        #pragma unroll
        for (int t = 0; t < 3; ++t) acc[i][t] = mfma(af, bf[t], acc[i][t]);
      }
    }
  }
  u32x4 qf[4][3];
  #pragma unroll
  for (int s = 0; s < 4; ++s)
    #pragma unroll
    for (int t = 0; t < 3; ++t) qf[s][t] = pack2(acc[2 * s][t], acc[2 * s + 1][t]);
  LAS char* K = lk + (w >> 1) * 24576;
  LAS char* V = lv + (w >> 1) * 24576;
  f32x4 sc[6][3];
  #pragma unroll
  for (int kt = 0; kt < 6; ++kt) {
    #pragma unroll
    for (int t = 0; t < 3; ++t) sc[kt][t] = f32x4{0, 0, 0, 0};
    #pragma unroll
    for (int s = 0; s < 4; ++s) {
      unsigned r = 16 * kt + c;
      u32x2 lo = *(LAS u32x2*)(K + r * 256 + (((4 * s + (g >> 1)) ^ c) << 4) + 8 * (g & 1));
      u32x2 hi = *(LAS u32x2*)(K + r * 256 + (((4 * s + 2 + (g >> 1)) ^ c) << 4) + 8 * (g & 1));
      u32x4 af = cat(lo, hi);
      #pragma unroll
      for (int t = 0; t < 3; ++t) sc[kt][t] = mfma(af, qf[s][t], sc[kt][t]);
    }
  }
  const float SC = 0.08838834764831845f * 1.4426950408889634f;
  float inv[3];
  u32x4 pf[3][3];
  #pragma unroll
  for (int t = 0; t < 3; ++t) {
    float m = sc[0][t][0];
    #pragma unroll
    for (int kt = 0; kt < 6; ++kt)
      #pragma unroll
      for (int r = 0; r < 4; ++r) m = fmaxf(m, sc[kt][t][r]);
    m = fmaxf(m, __shfl_xor(m, 16)); m = fmaxf(m, __shfl_xor(m, 32));
    float ms = m * SC, sum = 0;
    #pragma unroll
    for (int kt = 0; kt < 6; ++kt)
      #pragma unroll
      for (int r = 0; r < 4; ++r) { float e = __builtin_amdgcn_exp2f(sc[kt][t][r] * SC - ms); sc[kt][t][r] = e; sum += e; }
    sum += __shfl_xor(sum, 16); sum += __shfl_xor(sum, 32);
    inv[t] = 1.0f / sum;
    #pragma unroll
    for (int s = 0; s < 3; ++s) pf[s][t] = pack2(sc[2 * s][t], sc[2 * s + 1][t]);
  }
  f32x4 o[8][3];
  const unsigned fv = qq | ((g & 1) << 2);
  #pragma unroll
  for (int i = 0; i < 8; ++i) {
    #pragma unroll
    for (int t = 0; t < 3; ++t) o[i][t] = f32x4{0, 0, 0, 0};
    #pragma unroll
    for (int s = 0; s < 3; ++s) {
      LAS char* base = V + (32 * s + 4 * g + qq) * 256 + (((2 * i + (p >> 1)) ^ (fv << 1)) << 4) + 8 * (p & 1);
      u32x4 af = cat(trread(base), trread(base + 16 * 256));
      #pragma unroll
      for (int t = 0; t < 3; ++t) o[i][t] = mfma(af, pf[s][t], o[i][t]);
    }
  }
  char* O = a.ows + ((((size_t)br * 4 + bp) * 4 + w) * 32 + h) * 12288 + lane * 16;
  #pragma unroll
  for (int t = 0; t < 3; ++t)
    #pragma unroll
    for (int s = 0; s < 4; ++s) *(u32x4*)(O + (s * 3 + t) * 1024) = pack2(o[2 * s][t] * inv[t], o[2 * s + 1][t] * inv[t]);
}

__global__ __launch_bounds__(256, 1) void p2(Args a) {
  __shared__ __attribute__((aligned(16))) char smem[65536];
  LAS char* lds = (LAS char*)smem;
  const unsigned tid = threadIdx.x, lane = tid & 63, w = __builtin_amdgcn_readfirstlane(tid >> 6);
  const unsigned xcd = blockIdx.x & 7, idx = blockIdx.x >> 3;
  const unsigned br = xcd >> 2, qg = (xcd & 3) * 4 + (idx & 3), ns = idx >> 2;
  const unsigned c = lane & 15, g = lane >> 4, qq = c >> 2, p = lane & 3;
  const char* O = a.ows + (((size_t)br * 16 + qg) * 32 + w * 8) * 12288 + lane * 16;
  const char* W = a.wo[br] + (size_t)w * 1024 * 1280 + ns * 160;
  LAS char* lw = lds + w * 10240;
  f32x4 acc[5][3];
  #pragma unroll
  for (int m = 0; m < 5; ++m)
    #pragma unroll
    for (int t = 0; t < 3; ++t) acc[m][t] = f32x4{0, 0, 0, 0};
  unsigned goff[5];
  #pragma unroll
  for (int i = 0; i < 5; ++i) { unsigned n = lane + 64 * i; goff[i] = (n / 10) * 1280 + (n % 10) * 16; }
  u32x4 rw[5], bf[3];
  #pragma unroll
  for (int i = 0; i < 5; ++i) rw[i] = *(const u32x4*)(W + goff[i]);
  #pragma unroll
  for (int t = 0; t < 3; ++t) bf[t] = *(const u32x4*)(O + t * 1024);
  for (int ks = 0; ks < 32; ++ks) {
    LAS char* buf = lw + (ks & 1) * 5120;
    #pragma unroll
    for (int i = 0; i < 5; ++i) *(LAS u32x4*)(buf + (lane + 64 * i) * 16) = rw[i];
    u32x4 cb[3] = {bf[0], bf[1], bf[2]};
    int kn = ks < 31 ? ks + 1 : 31;
    #pragma unroll
    for (int i = 0; i < 5; ++i) rw[i] = *(const u32x4*)(W + (size_t)kn * 32 * 1280 + goff[i]);
    #pragma unroll
    for (int t = 0; t < 3; ++t) bf[t] = *(const u32x4*)(O + (kn * 3 + t) * 1024);
    __builtin_amdgcn_wave_barrier();
    #pragma unroll
    for (int m = 0; m < 5; ++m) {
      LAS char* base = buf + (4 * g + qq) * 160 + 32 * m + 8 * p;
      u32x4 af = cat(trread(base), trread(base + 16 * 160));
      #pragma unroll
      for (int t = 0; t < 3; ++t) acc[m][t] = mfma(af, cb[t], acc[m][t]);
    }
  }
  __syncthreads();
  #pragma unroll
  for (int m = 0; m < 5; ++m)
    #pragma unroll
    for (int t = 0; t < 3; ++t) *(LAS f32x4*)(lds + ((w * 15 + m * 3 + t) * 64 + lane) * 16) = acc[m][t];
  __syncthreads();
  for (int j = w; j < 15; j += 4) {
    int m = j / 3, t = j % 3;
    f32x4 s = f32x4{0, 0, 0, 0};
    #pragma unroll
    for (int u = 0; u < 4; ++u) s += *(LAS f32x4*)(lds + ((u * 15 + j) * 64 + lane) * 16);
    u32x2 r = {cvtpk(s[0], s[1]), cvtpk(s[2], s[3])};
    *(u32x2*)(a.out + (size_t)(qg * 48 + 16 * t + c) * 2560 + br * 1280 + (ns * 80 + 16 * m + 4 * g) * 2) = r;
  }
}

#define PROBE 0
struct PA { const uint4* p[10]; int n[10]; };
__global__ __launch_bounds__(256) void probe(PA a, uint4* sink) {
  uint4 acc = make_uint4(0,0,0,0);
  #pragma unroll
  for (int t = 0; t < 10; ++t) {
    int per = a.n[t] / gridDim.x;
    const uint4* p = a.p[t] + (size_t)blockIdx.x * per;
    #pragma unroll 4
    for (int i = threadIdx.x; i < per; i += 256) { uint4 v = p[i]; acc.x ^= v.x; acc.y ^= v.y; acc.z ^= v.z; acc.w ^= v.w; }
  }
  if ((acc.x ^ acc.y ^ acc.z ^ acc.w) == 0x12345677) sink[threadIdx.x] = acc;
}

extern "C" void kernel_launch(void* const* d_in, const int* in_sizes, int n_in,
                              void* d_out, int out_size, void* d_ws, size_t ws_size,
                              hipStream_t stream) {
  Args a;
  for (int i = 0; i < 2; ++i) {
    a.x[i] = (const char*)d_in[5 * i + 0]; a.k[i] = (const char*)d_in[5 * i + 1]; a.v[i] = (const char*)d_in[5 * i + 2];
    a.wq[i] = (const char*)d_in[5 * i + 3]; a.wo[i] = (const char*)d_in[5 * i + 4];
  }
  a.ows = (char*)d_ws; a.out = (char*)d_out;
  PA pa; for (int i = 0; i < 10; ++i) { pa.p[i] = (const uint4*)d_in[i]; pa.n[i] = in_sizes[i] / 8; }
  for (int i = 0; i < PROBE; ++i) probe<<<1024, 256, 0, stream>>>(pa, (uint4*)((char*)d_ws + (64 << 20)));
  hipFuncSetAttribute((const void*)p1, hipFuncAttributeMaxDynamicSharedMemorySize, 139264);
  p1<<<256, 256, 139264, stream>>>(a);
  p2<<<256, 256, 0, stream>>>(a);
}
```

```cpp
#include <hip/hip_runtime.h>
#include <hip/hip_bf16.h>
typedef unsigned u32x4 __attribute__((ext_vector_type(4)));
typedef unsigned u32x2 __attribute__((ext_vector_type(2)));
typedef short s16x4 __attribute__((ext_vector_type(4)));
typedef __bf16 bf16x8 __attribute__((ext_vector_type(8)));
typedef __bf16 bf16x2 __attribute__((ext_vector_type(2)));
typedef float f32x4 __attribute__((ext_vector_type(4)));
typedef float f32x2 __attribute__((ext_vector_type(2)));
typedef unsigned long long u64;
#define DI __device__ __forceinline__
#define LAS __attribute__((address_space(3)))
#define GAS __attribute__((address_space(1)))
#define SB0 __builtin_amdgcn_sched_barrier(0)
typedef __amdgpu_buffer_rsrc_t rsrc_t;
DI unsigned rfl(unsigned x) { return __builtin_amdgcn_readfirstlane(x); }
DI f32x4 mfma(u32x4 a, u32x4 b, f32x4 c) { return __builtin_amdgcn_mfma_f32_16x16x32_bf16(__builtin_bit_cast(bf16x8, a), __builtin_bit_cast(bf16x8, b), c, 0, 0, 0); }
DI unsigned cvtpk(float lo, float hi) { f32x2 v = {lo, hi}; bf16x2 b = __builtin_convertvector(v, bf16x2); return __builtin_bit_cast(unsigned, b); }
DI u32x2 trread(unsigned a) { return __builtin_bit_cast(u32x2, __builtin_amdgcn_ds_read_tr16_b64_v4i16((LAS s16x4*)a)); }
DI u32x4 cat(u32x2 a, u32x2 b) { u32x4 r = {a[0], a[1], b[0], b[1]}; return r; }
DI u32x4 pack2(f32x4 a, f32x4 b) { u32x4 r = {cvtpk(a[0], a[1]), cvtpk(a[2], a[3]), cvtpk(b[0], b[1]), cvtpk(b[2], b[3])}; return r; }
DI u32x4 lds128(unsigned a) { return *(LAS u32x4*)a; }
DI u32x2 lds64(unsigned a) { return *(LAS u32x2*)a; }
DI rsrc_t mkrsrc(const void* p, unsigned n) { return __builtin_amdgcn_make_buffer_rsrc((void*)p, 0, n, 0x20000); }
template<int N> DI void waitvm() { asm volatile("s_waitcnt vmcnt(%0)" :: "i"(N) : "memory"); }
DI void waitlgkm0() { asm volatile("s_waitcnt lgkmcnt(0)" ::: "memory"); }
DI void bar() { asm volatile("s_barrier" ::: "memory"); }
#define ABL 0
template<int NT> DI void dma(unsigned m0v, unsigned voff, rsrc_t r, unsigned soff) {
  if (ABL == 2) return;
  if (NT) asm volatile("s_mov_b32 m0, %0\n\ts_nop 0\n\tbuffer_load_dwordx4 %1, %2, %3 offen nt lds" :: "s"(m0v), "v"(voff), "s"(r), "s"(soff) : "memory");
  else    asm volatile("s_mov_b32 m0, %0\n\ts_nop 0\n\tbuffer_load_dwordx4 %1, %2, %3 offen lds" :: "s"(m0v), "v"(voff), "s"(r), "s"(soff) : "memory");
}
DI u32x4 ldnt(unsigned voff, rsrc_t r, unsigned soff) { u32x4 v; asm volatile("buffer_load_dwordx4 %0, %1, %2, %3 offen nt" : "=v"(v) : "v"(voff), "s"(r), "s"(soff) : "memory"); return v; }
DI float xmax16(float v) { auto r = __builtin_amdgcn_permlane16_swap(__float_as_uint(v), __float_as_uint(v), false, false); return fmaxf(__uint_as_float(r[0]), __uint_as_float(r[1])); }
DI float xmax32(float v) { auto r = __builtin_amdgcn_permlane32_swap(__float_as_uint(v), __float_as_uint(v), false, false); return fmaxf(__uint_as_float(r[0]), __uint_as_float(r[1])); }
DI float xadd16(float v) { auto r = __builtin_amdgcn_permlane16_swap(__float_as_uint(v), __float_as_uint(v), false, false); return __uint_as_float(r[0]) + __uint_as_float(r[1]); }
DI float xadd32(float v) { auto r = __builtin_amdgcn_permlane32_swap(__float_as_uint(v), __float_as_uint(v), false, false); return __uint_as_float(r[0]) + __uint_as_float(r[1]); }

#define STAMP(i) do {} while (0)

constexpr unsigned WS_O = 0;
constexpr unsigned WS_P = 12582912;
constexpr unsigned WS_F = WS_P + 2 * 4 * 8 * 4 * 3 * 4 * 2560;
constexpr unsigned FL = 128;
constexpr unsigned WS_F1 = WS_F;
constexpr unsigned WS_F2 = WS_F1 + 1024 * FL;
constexpr unsigned WS_N1 = WS_F2 + 1024 * FL;
constexpr unsigned WS_N2 = WS_N1 + 128 * FL;
constexpr unsigned WS_END = WS_N2 + 256 * FL;
constexpr unsigned WS_ST = 32u << 20;

constexpr unsigned L_K1 = 65536, L_K0 = 90112, L_V0 = 114688, L_V1 = 139264;
__host__ __device__ constexpr unsigned slot_of(int c) { return c % 3 == 0 ? 0u : (c % 3 == 1 ? 81920u : 40960u); }
constexpr unsigned SPIN_CAP = 1u << 17;

struct Args { const char* x[2]; const char* k[2]; const char* v[2]; const char* wq[2]; const char* wo[2]; char* ws; char* out; };

#define ROT 1
#define PW 0
#define PX 0
#define PO 0
#define FT 4
#define PR(cc) ((PW == 3 && (cc) >= 1 && (cc) <= FT) ? 1 : 0)
#define PRO 0
#define NC 2
#define RINGD 4
#define E2 0
#define KA 4
#define KB 7

template<int C> struct IC { static constexpr int value = C; };

__global__ __launch_bounds__(512, 2) void fused(Args a) {
  extern __shared__ __attribute__((aligned(16))) char smem[];
  const unsigned tid = threadIdx.x, lane = tid & 63, wv = rfl(tid >> 6), w = wv & 3;
  const unsigned xcd = blockIdx.x & 7, idx = blockIdx.x >> 3;
  const unsigned br = xcd >> 2, hg = xcd & 3, hl = idx & 7, h = hg * 8 + hl, bp = idx >> 3;
  const unsigned ns = (hl + 8 - 2 * bp) & 7;
  const unsigned c = lane & 15, g = lane >> 4, qq = c >> 2, p = lane & 3;
  const unsigned L0 = (unsigned)(size_t)(LAS char*)smem;
  char* ws = a.ws;
  STAMP(0);
  const char* pn1 = ws + WS_N1 + (((br * 4 + hg) * 4 + bp) * 4 + w) * FL;
  const char* pn2 = ws + WS_N2 + (((br * 4 + bp) * 8 + ns) * 4 + w) * FL;
  if (wv >= 4) {
    const rsrc_t rx = mkrsrc(a.x[br], 768 * 640 * 2), rw = mkrsrc(a.wq[br], 640 * 4096 * 2);
    const rsrc_t rk = mkrsrc(a.k[br], 8 * 32 * 96 * 128 * 2), rv = mkrsrc(a.v[br], 8 * 32 * 96 * 128 * 2), ro = mkrsrc(a.wo[br], 640 * 4096 * 2);
    const unsigned rot = ROT == 0 ? 0 : (ROT == 1 ? hl + ((5 * bp) >> 1) : (ROT == 2 ? ((5 * bp) >> 1) : (ROT == 3 ? hl : (ROT == 4 ? ((bp + hl) & 1) * 5 : ((5 * ((bp + hl) & 3)) >> 1)))));
    unsigned vx[2], vw[2], vv[2];
    #pragma unroll
    for (int i = 0; i < 2; ++i) {
      vx[i] = (lane >> 3) * 1280 + (((lane & 7) ^ ((4 * i + (lane >> 4)) & 7)) << 4);
      vw[i] = g * 8192 + h * 256 + ((c ^ ((g | (i << 2)) << 1)) << 4);
      vv[i] = g * 256 + ((c ^ ((g | (i << 2)) << 1)) << 4);
    }
    const unsigned sx = (bp * 192 + 48 * w) * 1280, sw = 16 * w * 8192;
    const unsigned skv0 = ((bp * 2) * 32 + h) * 24576 + w * 6144, skv1 = skv0 + 32 * 24576;
    unsigned pd[10];
    #pragma unroll
    for (int i = 0; i < 10; ++i) pd[i] = 0;
    auto chunk = [&](int cc) {
      unsigned kc;
      if (NC == 0) { kc = cc + rot; kc = kc >= 20 ? kc - 20 : (kc >= 10 ? kc - 10 : kc); }
      else if (cc < NC) kc = cc;
      else { kc = cc + (((10 - NC) * ((bp + hl) & 3)) >> 2); kc = kc >= 10 ? kc - (10 - NC) : kc; }
      const unsigned sb = L0 + slot_of(cc);
      #pragma unroll
      for (int i = 0; i < 4; ++i) {
        if (PW == 1 || (PW >= 2 && cc < FT)) dma<1>(sb + w * 4096 + i * 1024, vw[i >> 1], rw, sw + (kc * 64 + 4 * i) * 8192);
        else dma<0>(sb + w * 4096 + i * 1024, vw[i >> 1], rw, sw + (kc * 64 + 4 * i) * 8192);
      }
      if (PW == 3 && cc >= 1 && cc <= FT) {
        unsigned kp = cc - 1 + rot; kp = kp >= 20 ? kp - 20 : (kp >= 10 ? kp - 10 : kp);
        asm volatile("buffer_load_dword %0, %1, %2, %3 offen sc1" : "=v"(pd[cc]) : "v"(lane < 32 ? (lane >> 1) * 8192 + h * 256 + (lane & 1) * 128 : 0x80000000u), "s"(rw), "s"(sw + kp * 64 * 8192) : "memory");
      }
      #pragma unroll
      for (int i = 0; i < 6; ++i) dma<PX>(sb + 16384 + w * 6144 + i * 1024, vx[i & 1], rx, sx + i * 8 * 1280 + kc * 128);
    };
    u32x4 kr[2][6];
    auto kload = [&](int b) {
      #pragma unroll
      for (int i = 0; i < 6; ++i) kr[b][i] = ldnt(lane * 16, rk, (b ? skv1 : skv0) + i * 1024);
    };
    auto kwrite = [&](int b) {
      #pragma unroll
      for (int i = 0; i < 6; ++i) {
        asm volatile("" : "+v"(kr[b][i]));
        const unsigned row = 24 * w + 4 * i + g;
        *(LAS u32x4*)(L0 + (b ? L_K1 : L_K0) + row * 256 + ((c ^ (row & 15)) << 4)) = kr[b][i];
      }
    };
    auto vdma = [&](int b) {
      #pragma unroll
      for (int i = 0; i < 6; ++i) dma<1>(L0 + (b ? L_V1 : L_V0) + w * 6144 + i * 1024, vv[i & 1], rv, (b ? skv1 : skv0) + i * 1024);
    };
    unsigned vo[5];
    #pragma unroll
    for (int i = 0; i < 5; ++i) { const unsigned n = lane + 64 * i; vo[i] = (n / 10) * 1280 + (n % 10) * 16 + ns * 160; }
    auto wo_blocks = [&](int o0, int o1) {
      #pragma unroll
      for (int o = o0; o < o1; o += 4) {
        const unsigned oo = o + w;
        if (oo < (unsigned)o1) {
          const unsigned hh = (hl + (oo >> 2)) & 7;
          const unsigned so = ((hg * 8 + hh) * 128 + (oo & 3) * 32) * 1280;
          #pragma unroll
          for (int i = 0; i < 5; ++i) dma<PO>(L0 + oo * 5120 + i * 1024, vo[i], ro, so);
        }
      }
    };
    chunk(0); chunk(1);
    #define EXTRA(cc) (((KA >= (cc) && KA <= (cc) + 1) ? 6 : 0) + ((KB >= (cc) && KB <= (cc) + 1) ? 6 : 0))
    u64 tag1;
    asm volatile("s_load_dwordx2 %0, %1, 0x0 glc\n\ts_waitcnt vmcnt(%2)\n\ts_waitcnt lgkmcnt(0)" : "=&s"(tag1) : "s"(pn1), "i"(10 + PR(1)) : "memory");
    tag1 += 1;
    bar(); STAMP(1);
    chunk(2); if (KA == 2) kload(0);
    waitvm<10 + PR(2) + (KA == 2 ? 6 : 0)>(); bar(); STAMP(2);
    auto step = [&](auto C) {
      constexpr int cc = C.value;
      chunk(cc + 1);
      if (KA == cc + 1) kload(0);
      if (KB == cc + 1) kload(1);
      waitvm<10 + PR(cc + 1) + EXTRA(cc)>(); bar(); STAMP(cc + 1);
    };
    step(IC<2>{}); step(IC<3>{}); step(IC<4>{}); step(IC<5>{}); step(IC<6>{}); step(IC<7>{});
    chunk(9); vdma(1);
    waitvm<16>(); bar(); STAMP(9);
    vdma(0); kwrite(0);
    waitvm<12>(); bar(); STAMP(10);
    kwrite(1); waitlgkm0(); bar(); STAMP(11);
    if (PW == 3) {
      #pragma unroll
      for (int i = 0; i < 10; ++i) asm volatile("" :: "v"(pd[i]));
    }
    wo_blocks(0, 4);
    waitvm<5>(); STAMP(12);
    wo_blocks(4, 8);
    bar(); STAMP(13);
    wo_blocks(8, 12);
    bar(); STAMP(14);
    wo_blocks(12, 32);
    {
      const unsigned peer = (bp * 8 + ((hl + 1 + lane % 7) & 7)) * 8 + xcd;
      GAS u64* f = (GAS u64*)(ws + WS_F1 + (peer * 4 + w) * FL);
      STAMP(15);
      for (unsigned s = 0; s < SPIN_CAP; ++s) {
        const u64 v = __hip_atomic_load(f, __ATOMIC_RELAXED, __HIP_MEMORY_SCOPE_AGENT);
        if (__all(v == tag1)) break;
        __builtin_amdgcn_s_sleep(2);
      }
    }
    waitvm<0>(); STAMP(16);
    bar();
    if (hl == 0 && lane == 0) __hip_atomic_store((GAS u64*)(ws + WS_N1 + (((br * 4 + hg) * 4 + bp) * 4 + w) * FL), tag1, __ATOMIC_RELAXED, __HIP_MEMORY_SCOPE_AGENT);
  } else {
    const unsigned f8 = qq | ((g & 1) << 2);
    unsigned aA[8], aB[2];
    #pragma unroll
    for (int i = 0; i < 8; ++i) aA[i] = L0 + (8 * g + qq) * 256 + (((2 * i + (p >> 1)) ^ (f8 << 1)) << 4) + 8 * (p & 1);
    #pragma unroll
    for (int s = 0; s < 2; ++s) aB[s] = L0 + 16384 + (w * 48 + c) * 128 + (((4 * s + g) ^ ((c >> 1) & 7)) << 4);
    f32x4 acc[8][3];
    #pragma unroll
    for (int i = 0; i < 8; ++i)
      #pragma unroll
      for (int t = 0; t < 3; ++t) acc[i][t] = f32x4{0, 0, 0, 0};
    u32x4 A0[8], B0[3], A1[8], B1[3];
    auto rdA = [&](u32x4* A, unsigned sb, int s) {
      #pragma unroll
      for (int i = 0; i < 8; ++i) A[i] = cat(trread(aA[i] + sb + s * 8192), trread(aA[i] + sb + s * 8192 + 1024));
    };
    auto rdB = [&](u32x4* B, unsigned sb, int s) {
      #pragma unroll
      for (int t = 0; t < 3; ++t) B[t] = lds128(aB[s] + sb + t * 2048);
    };
    auto mm = [&](u32x4* A, u32x4* B) {
      #pragma unroll
      for (int i = 0; i < 8; ++i)
        #pragma unroll
        for (int t = 0; t < 3; ++t) acc[i][t] = mfma(A[i], B[t], acc[i][t]);
    };
    auto interleave = [&]() {
      #pragma unroll
      for (int i = 0; i < 19; ++i) { __builtin_amdgcn_sched_group_barrier(0x8, 1, 0); __builtin_amdgcn_sched_group_barrier(0x100, 1, 0); }
      __builtin_amdgcn_sched_group_barrier(0x8, 5, 0);
    };
    u64 tag1, tag2;
    asm volatile("s_load_dwordx2 %0, %2, 0x0 glc\n\ts_load_dwordx2 %1, %3, 0x0 glc\n\ts_waitcnt lgkmcnt(0)" : "=&s"(tag1), "=&s"(tag2) : "s"(pn1), "s"(pn2) : "memory");
    tag1 += 1; tag2 += 1;
    bar(); STAMP(1);
    rdB(B0, 0, 0); SB0; rdA(A0, 0, 0); SB0;
    unsigned sb = 0;
    for (int cc = 0; cc < 10; ++cc) {
      if (ABL == 1) { bar(); continue; }
      rdB(B1, sb, 1); rdA(A1, sb, 1);
      mm(A0, B0);
      interleave();
      SB0; waitlgkm0(); bar(); SB0;
      sb = sb == 0 ? 81920u : (sb == 81920u ? 40960u : 0u);
      if (cc < 9) { rdB(B0, sb, 0); rdA(A0, sb, 0); }
      mm(A1, B1);
      interleave();
    }
    STAMP(11);
    u32x4 qf[4][3];
    #pragma unroll
    for (int s = 0; s < 4; ++s)
      #pragma unroll
      for (int t = 0; t < 3; ++t) qf[s][t] = pack2(acc[2 * s][t], acc[2 * s + 1][t]);
    const unsigned Kb = L0 + ((w >> 1) ? L_K1 : L_K0) + c * 256 + 8 * (g & 1);
    f32x4 sc[6][3];
    #pragma unroll
    for (int kt = 0; kt < 6; ++kt) {
      #pragma unroll
      for (int t = 0; t < 3; ++t) sc[kt][t] = f32x4{0, 0, 0, 0};
      #pragma unroll
      for (int s = 0; s < 4; ++s) {
        const u32x4 af = cat(lds64(Kb + kt * 4096 + (((4 * s + (g >> 1)) ^ c) << 4)), lds64(Kb + kt * 4096 + (((4 * s + 2 + (g >> 1)) ^ c) << 4)));
        #pragma unroll
        for (int t = 0; t < 3; ++t) sc[kt][t] = mfma(af, qf[s][t], sc[kt][t]);
      }
    }
    const float SC = 0.08838834764831845f * 1.4426950408889634f;
    float inv[3];
    u32x4 pf[3][3];
    #pragma unroll
    for (int t = 0; t < 3; ++t) {
      float m = sc[0][t][0];
      #pragma unroll
      for (int kt = 0; kt < 6; ++kt)
        #pragma unroll
        for (int r = 0; r < 4; ++r) m = fmaxf(m, sc[kt][t][r]);
      m = xmax32(xmax16(m));
      const float ms = m * SC; float sum = 0;
      #pragma unroll
      for (int kt = 0; kt < 6; ++kt)
        #pragma unroll
        for (int r = 0; r < 4; ++r) { const float e = __builtin_amdgcn_exp2f(sc[kt][t][r] * SC - ms); sc[kt][t][r] = e; sum += e; }
      sum = xadd32(xadd16(sum));
      inv[t] = __builtin_amdgcn_rcpf(sum);
      #pragma unroll
      for (int s = 0; s < 3; ++s) pf[s][t] = pack2(sc[2 * s][t], sc[2 * s + 1][t]);
    }
    SB0; STAMP(13);
    waitlgkm0(); bar(); SB0;
    STAMP(14);
    const unsigned Vb = L0 + ((w >> 1) ? L_V1 : L_V0) + (4 * g + qq) * 256 + 8 * (p & 1);
    u32x4 of[4][3];
    {
      f32x4 o[8][3];
      #pragma unroll
      for (int i = 0; i < 8; ++i) {
        #pragma unroll
        for (int t = 0; t < 3; ++t) o[i][t] = f32x4{0, 0, 0, 0};
        const unsigned vb = Vb + (((2 * i + (p >> 1)) ^ (f8 << 1)) << 4);
        #pragma unroll
        for (int s = 0; s < 3; ++s) {
          const u32x4 af = cat(trread(vb + s * 8192), trread(vb + s * 8192 + 4096));
          #pragma unroll
          for (int t = 0; t < 3; ++t) o[i][t] = mfma(af, pf[s][t], o[i][t]);
        }
      }
      #pragma unroll
      for (int s = 0; s < 4; ++s)
        #pragma unroll
        for (int t = 0; t < 3; ++t) of[s][t] = pack2(o[2 * s][t] * inv[t], o[2 * s + 1][t] * inv[t]);
    }
    const rsrc_t rO = mkrsrc(ws + WS_O + (size_t)((br * 4 + bp) * 4 + w) * 32 * 12288, 32 * 12288);
    #pragma unroll
    for (int s = 0; s < 4; ++s)
      #pragma unroll
      for (int t = 0; t < 3; ++t) __builtin_amdgcn_raw_buffer_store_b128(of[s][t], rO, lane * 16 + (s * 3 + t) * 1024, h * 12288, 16);
    SB0; bar(); SB0;
    STAMP(15);
    waitvm<0>();
    if (lane == 0) __hip_atomic_store((GAS u64*)(ws + WS_F1 + (blockIdx.x * 4 + w) * FL), tag1, __ATOMIC_RELAXED, __HIP_MEMORY_SCOPE_AGENT);
    STAMP(16);
    bar();
    STAMP(17);
    f32x4 oc[5][3];
    #pragma unroll
    for (int m = 0; m < 5; ++m)
      #pragma unroll
      for (int t = 0; t < 3; ++t) oc[m][t] = f32x4{0, 0, 0, 0};
    constexpr int RING = RINGD;
    u32x4 bq[RING][3];
    const unsigned aW = L0 + (4 * g + qq) * 160 + 8 * p;
    const unsigned vl = lane * 16;
    auto bload = [&](int o) {
      const unsigned so = (hg * 8 + ((hl + (E2 == 1 ? 1 : (o >> 2))) & 7)) * 12288 + (o & 3) * 3072;
      if (E2 == 2) { bq[o % RING][0] = of[o & 3][0]; bq[o % RING][1] = of[o & 3][1]; bq[o % RING][2] = of[o & 3][2]; return; }
      asm volatile("buffer_load_dwordx4 %0, %1, %2, %3 offen sc1" : "=v"(bq[o % RING][0]) : "v"(vl), "s"(rO), "s"(so) : "memory");
      asm volatile("buffer_load_dwordx4 %0, %1, %2, %3 offen offset:1024 sc1" : "=v"(bq[o % RING][1]) : "v"(vl), "s"(rO), "s"(so) : "memory");
      asm volatile("buffer_load_dwordx4 %0, %1, %2, %3 offen offset:2048 sc1" : "=v"(bq[o % RING][2]) : "v"(vl), "s"(rO), "s"(so) : "memory");
    };
    u32x4 W[2][5];
    auto rdW = [&](int o) {
      #pragma unroll
      for (int m = 0; m < 5; ++m) {
        const unsigned ad = aW + (o >= 12 ? (o >= 24 ? 24 : 12) : 0) * 5120;
        const unsigned off = (o - (o >= 12 ? (o >= 24 ? 24 : 12) : 0)) * 5120 + 32 * m;
        W[o & 1][m] = cat(trread(ad + off), trread(ad + off + 16 * 160));
      }
    };
    auto kstep = [&](int o, const u32x4* B) {
      if (o < 31) rdW(o + 1);
      #pragma unroll
      for (int m = 0; m < 5; ++m)
        #pragma unroll
        for (int t = 0; t < 3; ++t) oc[m][t] = mfma(W[o & 1][m], B[t], oc[m][t]);
      #pragma unroll
      for (int i = 0; i < 10; ++i) { __builtin_amdgcn_sched_group_barrier(0x8, 1, 0); __builtin_amdgcn_sched_group_barrier(0x100, 1, 0); }
      __builtin_amdgcn_sched_group_barrier(0x8, 5, 0);
      SB0;
    };
    rdW(0);
    #pragma unroll
    for (int o = 4; o < 4 + RING - 1; ++o) bload(o);
    SB0;
    #pragma unroll
    for (int o = 0; o < 4; ++o) kstep(o, of[o]);
    auto pstep = [&](auto O) {
      constexpr int o = O.value;
      if (o + RING - 1 < 32) bload(o + RING - 1);
      constexpr int later = (o + RING - 1 < 32 ? RING - 1 : 31 - o) * 3;
      asm volatile("s_waitcnt vmcnt(%3)" : "+v"(bq[o % RING][0]), "+v"(bq[o % RING][1]), "+v"(bq[o % RING][2]) : "i"(later) : "memory");
      SB0;
      kstep(o, bq[o % RING]);
    };
    pstep(IC<4>{}); pstep(IC<5>{}); pstep(IC<6>{}); pstep(IC<7>{}); pstep(IC<8>{}); pstep(IC<9>{}); pstep(IC<10>{}); pstep(IC<11>{});
    pstep(IC<12>{}); pstep(IC<13>{}); pstep(IC<14>{}); pstep(IC<15>{}); pstep(IC<16>{}); pstep(IC<17>{}); pstep(IC<18>{}); pstep(IC<19>{});
    pstep(IC<20>{}); pstep(IC<21>{}); pstep(IC<22>{}); pstep(IC<23>{}); pstep(IC<24>{}); pstep(IC<25>{}); pstep(IC<26>{}); pstep(IC<27>{});
    pstep(IC<28>{}); pstep(IC<29>{}); pstep(IC<30>{}); pstep(IC<31>{});
    STAMP(18);
    #pragma unroll
    for (int t = 0; t < 3; ++t)
      #pragma unroll
      for (int m = 0; m < 4; m += 2)
        #pragma unroll
        for (int r = 0; r < 4; ++r) {
          auto x = __builtin_amdgcn_permlane16_swap(__float_as_uint(oc[m][t][r]), __float_as_uint(oc[m + 1][t][r]), false, false);
          oc[m][t][r] = __uint_as_float(x[0]); oc[m + 1][t][r] = __uint_as_float(x[1]);
        }
    f32x4 e4[3];
    #pragma unroll
    for (int t = 0; t < 3; ++t)
      #pragma unroll
      for (int r = 0; r < 4; ++r) {
        auto x = __builtin_amdgcn_permlane16_swap(__float_as_uint(oc[4][t][r]), __float_as_uint(oc[4][t][r]), false, false);
        oc[4][t][r] = __uint_as_float(x[0]); e4[t][r] = __uint_as_float(x[1]);
      }
    const rsrc_t rP = mkrsrc(ws + WS_P + (size_t)((((br * 4 + bp) * 8 + ns) * 4 + w) * 3) * 4 * 2560, 3 * 4 * 2560);
    const unsigned v4 = (g & 1) ? 0x80000000u : 2048 + ((g >> 1) * 16 + c) * 16;
    const unsigned tme = (hg + 3 - w) & 3;
    #pragma unroll
    for (int t = 0; t < 3; ++t) {
      if (tme != (unsigned)t) {
        const unsigned so = (t * 4 + hg) * 2560;
        __builtin_amdgcn_raw_buffer_store_b128(pack2(oc[0][t], oc[1][t]), rP, lane * 16, so, 16);
        __builtin_amdgcn_raw_buffer_store_b128(pack2(oc[2][t], oc[3][t]), rP, lane * 16 + 1024, so, 16);
        __builtin_amdgcn_raw_buffer_store_b128(pack2(oc[4][t], e4[t]), rP, v4, so, 16);
      }
    }
    waitvm<0>();
    if (lane == 0) __hip_atomic_store((GAS u64*)(ws + WS_F2 + (blockIdx.x * 4 + w) * FL), tag2, __ATOMIC_RELAXED, __HIP_MEMORY_SCOPE_AGENT);
    STAMP(19);
    if (tme < 3) {
      const unsigned peer = idx * 8 + br * 4 + ((hg + 1 + lane % 3) & 3);
      GAS u64* f = (GAS u64*)(ws + WS_F2 + (peer * 4 + w) * FL);
      for (unsigned s = 0; s < SPIN_CAP; ++s) {
        const u64 v = __hip_atomic_load(f, __ATOMIC_RELAXED, __HIP_MEMORY_SCOPE_AGENT);
        if (__all(v == tag2)) break;
        __builtin_amdgcn_s_sleep(1);
      }
      asm volatile("" ::: "memory");
      STAMP(20);
      u32x4 in[3][3];
      #pragma unroll
      for (int j = 0; j < 3; ++j) {
        const unsigned so = (tme * 4 + ((hg + 1 + j) & 3)) * 2560;
        in[j][0] = __builtin_amdgcn_raw_buffer_load_b128(rP, lane * 16, so, 16);
        in[j][1] = __builtin_amdgcn_raw_buffer_load_b128(rP, lane * 16 + 1024, so, 16);
        in[j][2] = __builtin_amdgcn_raw_buffer_load_b128(rP, v4, so, 16);
      }
      f32x4 y[6];
      #pragma unroll
      for (int m = 0; m < 5; ++m) y[m] = tme == 0 ? oc[m][0] : (tme == 1 ? oc[m][1] : oc[m][2]);
      y[5] = tme == 0 ? e4[0] : (tme == 1 ? e4[1] : e4[2]);
      #pragma unroll
      for (int j = 0; j < 3; ++j)
        #pragma unroll
        for (int u = 0; u < 3; ++u)
          #pragma unroll
          for (int d = 0; d < 4; ++d) {
            y[2 * u + (d >> 1)][2 * (d & 1)] += __uint_as_float(in[j][u][d] << 16);
            y[2 * u + (d >> 1)][2 * (d & 1) + 1] += __uint_as_float(in[j][u][d] & 0xffff0000u);
          }
      const rsrc_t rY = mkrsrc(a.out + (size_t)(bp * 192 + w * 48 + tme * 16) * 2560 + br * 1280 + ns * 160, 16 * 2560);
      const unsigned vy = c * 2560 + (g & 1) * 32 + (g >> 1) * 16;
      __builtin_amdgcn_raw_buffer_store_b128(pack2(y[0], y[1]), rY, vy, 0, 0);
      __builtin_amdgcn_raw_buffer_store_b128(pack2(y[2], y[3]), rY, vy + 64, 0, 0);
      __builtin_amdgcn_raw_buffer_store_b128(pack2(y[4], y[5]), rY, (g & 1) ? 0x80000000u : c * 2560 + 128 + (g >> 1) * 16, 0, 0);
      if (tme == 0 && lane == 0) __hip_atomic_store((GAS u64*)(ws + WS_N2 + (((br * 4 + bp) * 8 + ns) * 4 + w) * FL), tag2, __ATOMIC_RELAXED, __HIP_MEMORY_SCOPE_AGENT);
    }
    STAMP(21);
  }
}

extern "C" void kernel_launch(void* const* d_in, const int* in_sizes, int n_in,
                              void* d_out, int out_size, void* d_ws, size_t ws_size,
                              hipStream_t stream) {
  Args a;
  for (int i = 0; i < 2; ++i) {
    a.x[i] = (const char*)d_in[5 * i + 0]; a.k[i] = (const char*)d_in[5 * i + 1]; a.v[i] = (const char*)d_in[5 * i + 2];
    a.wq[i] = (const char*)d_in[5 * i + 3]; a.wo[i] = (const char*)d_in[5 * i + 4];
  }
  a.ws = (char*)d_ws; a.out = (char*)d_out;
  (void)hipFuncSetAttribute((const void*)fused, hipFuncAttributeMaxDynamicSharedMemorySize, 163840);
  fused<<<256, 512, 163840, stream>>>(a);
}
```

```cpp
#include <hip/hip_runtime.h>
#include <hip/hip_bf16.h>
typedef unsigned u32x4 __attribute__((ext_vector_type(4)));
typedef unsigned u32x2 __attribute__((ext_vector_type(2)));
typedef short s16x4 __attribute__((ext_vector_type(4)));
typedef __bf16 bf16x8 __attribute__((ext_vector_type(8)));
typedef __bf16 bf16x2 __attribute__((ext_vector_type(2)));
typedef float f32x4 __attribute__((ext_vector_type(4)));
typedef float f32x2 __attribute__((ext_vector_type(2)));
typedef unsigned long long u64;
#define DI __device__ __forceinline__
#define LAS __attribute__((address_space(3)))
#define GAS __attribute__((address_space(1)))
#define SB0 __builtin_amdgcn_sched_barrier(0)
typedef __amdgpu_buffer_rsrc_t rsrc_t;
DI unsigned rfl(unsigned x) { return __builtin_amdgcn_readfirstlane(x); }
DI f32x4 mfma(u32x4 a, u32x4 b, f32x4 c) { return __builtin_amdgcn_mfma_f32_16x16x32_bf16(__builtin_bit_cast(bf16x8, a), __builtin_bit_cast(bf16x8, b), c, 0, 0, 0); }
DI unsigned cvtpk(float lo, float hi) { f32x2 v = {lo, hi}; bf16x2 b = __builtin_convertvector(v, bf16x2); return __builtin_bit_cast(unsigned, b); }
DI u32x2 trread(unsigned a) { return __builtin_bit_cast(u32x2, __builtin_amdgcn_ds_read_tr16_b64_v4i16((LAS s16x4*)a)); }
DI u32x4 cat(u32x2 a, u32x2 b) { u32x4 r = {a[0], a[1], b[0], b[1]}; return r; }
DI u32x4 pack2(f32x4 a, f32x4 b) { u32x4 r = {cvtpk(a[0], a[1]), cvtpk(a[2], a[3]), cvtpk(b[0], b[1]), cvtpk(b[2], b[3])}; return r; }
DI u32x4 lds128(unsigned a) { return *(LAS u32x4*)a; }
DI u32x2 lds64(unsigned a) { return *(LAS u32x2*)a; }
DI rsrc_t mkrsrc(const void* p, unsigned n) { return __builtin_amdgcn_make_buffer_rsrc((void*)p, 0, n, 0x20000); }
template<int N> DI void waitvm() { asm volatile("s_waitcnt vmcnt(%0)" :: "i"(N) : "memory"); }
DI void waitlgkm0() { asm volatile("s_waitcnt lgkmcnt(0)" ::: "memory"); }
DI void bar() { asm volatile("s_barrier" ::: "memory"); }
#define ABL 0
template<int NT> DI void dma(unsigned m0v, unsigned voff, rsrc_t r, unsigned soff) {
  if (ABL == 2) return;
  if (NT) asm volatile("s_mov_b32 m0, %0\n\ts_nop 0\n\tbuffer_load_dwordx4 %1, %2, %3 offen nt lds" :: "s"(m0v), "v"(voff), "s"(r), "s"(soff) : "memory");
  else    asm volatile("s_mov_b32 m0, %0\n\ts_nop 0\n\tbuffer_load_dwordx4 %1, %2, %3 offen lds" :: "s"(m0v), "v"(voff), "s"(r), "s"(soff) : "memory");
}
#define PK 1
DI u32x4 ldnt(unsigned voff, rsrc_t r, unsigned soff) { u32x4 v;
  if (PK) asm volatile("buffer_load_dwordx4 %0, %1, %2, %3 offen nt" : "=v"(v) : "v"(voff), "s"(r), "s"(soff) : "memory");
  else asm volatile("buffer_load_dwordx4 %0, %1, %2, %3 offen" : "=v"(v) : "v"(voff), "s"(r), "s"(soff) : "memory");
  return v; }
DI float xmax16(float v) { auto r = __builtin_amdgcn_permlane16_swap(__float_as_uint(v), __float_as_uint(v), false, false); return fmaxf(__uint_as_float(r[0]), __uint_as_float(r[1])); }
DI float xmax32(float v) { auto r = __builtin_amdgcn_permlane32_swap(__float_as_uint(v), __float_as_uint(v), false, false); return fmaxf(__uint_as_float(r[0]), __uint_as_float(r[1])); }
DI float xadd16(float v) { auto r = __builtin_amdgcn_permlane16_swap(__float_as_uint(v), __float_as_uint(v), false, false); return __uint_as_float(r[0]) + __uint_as_float(r[1]); }
DI float xadd32(float v) { auto r = __builtin_amdgcn_permlane32_swap(__float_as_uint(v), __float_as_uint(v), false, false); return __uint_as_float(r[0]) + __uint_as_float(r[1]); }

#define STAMP(i) do {} while (0)

constexpr unsigned WS_O = 0;
constexpr unsigned WS_P = 12582912;
constexpr unsigned WS_F = WS_P + 2 * 4 * 8 * 4 * 3 * 4 * 2560;
constexpr unsigned FL = 128;
constexpr unsigned WS_F1 = WS_F;
constexpr unsigned WS_F2 = WS_F1 + 1024 * FL;
constexpr unsigned WS_N1 = WS_F2 + 1024 * FL;
constexpr unsigned WS_N2 = WS_N1 + 128 * FL;
constexpr unsigned WS_END = WS_N2 + 256 * FL;
constexpr unsigned WS_ST = 32u << 20;

constexpr unsigned L_K1 = 65536, L_K0 = 90112, L_V0 = 114688, L_V1 = 139264;
__host__ __device__ constexpr unsigned slot_of(int c) { return c % 3 == 0 ? 0u : (c % 3 == 1 ? 81920u : 40960u); }
constexpr unsigned SPIN_CAP = 1u << 17;

struct Args { const char* x[2]; const char* k[2]; const char* v[2]; const char* wq[2]; const char* wo[2]; char* ws; char* out; };

#define ROT 1
#define PW 0
#define PX 0
#define PO 0
#define FT 4
#define PR(cc) ((PW == 3 && (cc) >= 1 && (cc) <= FT) ? 1 : 0)
#define PRO 0
#define NC 2
#define RINGD 4
#define E2 0
#define KA 4
#define KB 7

template<int C> struct IC { static constexpr int value = C; };

__global__ __launch_bounds__(512, 2) void fused(Args a) {
  extern __shared__ __attribute__((aligned(16))) char smem[];
  const unsigned tid = threadIdx.x, lane = tid & 63, wv = rfl(tid >> 6), w = wv & 3;
  const unsigned xcd = blockIdx.x & 7, idx = blockIdx.x >> 3;
  const unsigned br = xcd >> 2, hg = xcd & 3, hl = idx & 7, h = hg * 8 + hl, bp = idx >> 3;
  const unsigned ns = (hl + 8 - 2 * bp) & 7;
  const unsigned c = lane & 15, g = lane >> 4, qq = c >> 2, p = lane & 3;
  const unsigned L0 = (unsigned)(size_t)(LAS char*)smem;
  char* ws = a.ws;
  STAMP(0);
  const char* pn1 = ws + WS_N1 + (((br * 4 + hg) * 4 + bp) * 4 + w) * FL;
  const char* pn2 = ws + WS_N2 + (((br * 4 + bp) * 8 + ns) * 4 + w) * FL;
  if (wv >= 4) {
    const rsrc_t rx = mkrsrc(a.x[br], 768 * 640 * 2), rw = mkrsrc(a.wq[br], 640 * 4096 * 2);
    const rsrc_t rk = mkrsrc(a.k[br], 8 * 32 * 96 * 128 * 2), rv = mkrsrc(a.v[br], 8 * 32 * 96 * 128 * 2), ro = mkrsrc(a.wo[br], 640 * 4096 * 2);
    const unsigned rot = ROT == 0 ? 0 : (ROT == 1 ? hl + ((5 * bp) >> 1) : (ROT == 2 ? ((5 * bp) >> 1) : (ROT == 3 ? hl : (ROT == 4 ? ((bp + hl) & 1) * 5 : ((5 * ((bp + hl) & 3)) >> 1)))));
    unsigned vx[2], vw[2], vv[2];
    #pragma unroll
    for (int i = 0; i < 2; ++i) {
      vx[i] = (lane >> 3) * 1280 + (((lane & 7) ^ ((4 * i + (lane >> 4)) & 7)) << 4);
      vw[i] = g * 8192 + h * 256 + ((c ^ ((g | (i << 2)) << 1)) << 4);
      vv[i] = g * 256 + ((c ^ ((g | (i << 2)) << 1)) << 4);
    }
    const unsigned sx = (bp * 192 + 48 * w) * 1280, sw = 16 * w * 8192;
    const unsigned skv0 = ((bp * 2) * 32 + h) * 24576 + w * 6144, skv1 = skv0 + 32 * 24576;
    unsigned pd[10];
    #pragma unroll
    for (int i = 0; i < 10; ++i) pd[i] = 0;
    auto chunk = [&](int cc) {
      unsigned kc;
      if (NC == 0) { kc = cc + rot; kc = kc >= 20 ? kc - 20 : (kc >= 10 ? kc - 10 : kc); }
      else if (cc < NC) kc = cc;
      else { kc = cc + (((10 - NC) * ((bp + hl) & 3)) >> 2); kc = kc >= 10 ? kc - (10 - NC) : kc; }
      const unsigned sb = L0 + slot_of(cc);
      #pragma unroll
      for (int i = 0; i < 4; ++i) {
        if (PW == 1 || (PW >= 2 && cc < FT)) dma<1>(sb + w * 4096 + i * 1024, vw[i >> 1], rw, sw + (kc * 64 + 4 * i) * 8192);
        else dma<0>(sb + w * 4096 + i * 1024, vw[i >> 1], rw, sw + (kc * 64 + 4 * i) * 8192);
      }
      if (PW == 3 && cc >= 1 && cc <= FT) {
        unsigned kp = cc - 1 + rot; kp = kp >= 20 ? kp - 20 : (kp >= 10 ? kp - 10 : kp);
        asm volatile("buffer_load_dword %0, %1, %2, %3 offen sc1" : "=v"(pd[cc]) : "v"(lane < 32 ? (lane >> 1) * 8192 + h * 256 + (lane & 1) * 128 : 0x80000000u), "s"(rw), "s"(sw + kp * 64 * 8192) : "memory");
      }
      #pragma unroll
      for (int i = 0; i < 6; ++i) dma<PX>(sb + 16384 + w * 6144 + i * 1024, vx[i & 1], rx, sx + i * 8 * 1280 + kc * 128);
    };
    u32x4 kr[2][6];
    auto kload = [&](int b) {
      #pragma unroll
      for (int i = 0; i < 6; ++i) kr[b][i] = ldnt(lane * 16, rk, (b ? skv1 : skv0) + i * 1024);
    };
    auto kwrite = [&](int b) {
      #pragma unroll
      for (int i = 0; i < 6; ++i) {
        asm volatile("" : "+v"(kr[b][i]));
        const unsigned row = 24 * w + 4 * i + g;
        *(LAS u32x4*)(L0 + (b ? L_K1 : L_K0) + row * 256 + ((c ^ (row & 15)) << 4)) = kr[b][i];
      }
    };
    auto vdma = [&](int b) {
      #pragma unroll
      for (int i = 0; i < 6; ++i) dma<PK>(L0 + (b ? L_V1 : L_V0) + w * 6144 + i * 1024, vv[i & 1], rv, (b ? skv1 : skv0) + i * 1024);
    };
    unsigned vo[5];
    #pragma unroll
    for (int i = 0; i < 5; ++i) { const unsigned n = lane + 64 * i; vo[i] = (n / 10) * 1280 + (n % 10) * 16 + ns * 160; }
    auto wo_blocks = [&](int o0, int o1) {
      #pragma unroll
      for (int o = o0; o < o1; o += 4) {
        const unsigned oo = o + w;
        if (oo < (unsigned)o1) {
          const unsigned hh = (hl + (oo >> 2)) & 7;
          const unsigned so = ((hg * 8 + hh) * 128 + (oo & 3) * 32) * 1280;
          #pragma unroll
          for (int i = 0; i < 5; ++i) dma<PO>(L0 + oo * 5120 + i * 1024, vo[i], ro, so);
        }
      }
    };
    chunk(0); chunk(1);
    #define EXTRA(cc) (((KA >= (cc) && KA <= (cc) + 1) ? 6 : 0) + ((KB >= (cc) && KB <= (cc) + 1) ? 6 : 0))
    u64 tag1;
    asm volatile("s_load_dwordx2 %0, %1, 0x0 glc\n\ts_waitcnt vmcnt(%2)\n\ts_waitcnt lgkmcnt(0)" : "=&s"(tag1) : "s"(pn1), "i"(10 + PR(1)) : "memory");
    tag1 += 1;
    bar(); STAMP(1);
    chunk(2); if (KA == 2) kload(0);
    waitvm<10 + PR(2) + (KA == 2 ? 6 : 0)>(); bar(); STAMP(2);
    auto step = [&](auto C) {
      constexpr int cc = C.value;
      chunk(cc + 1);
      if (KA == cc + 1) kload(0);
      if (KB == cc + 1) kload(1);
      waitvm<10 + PR(cc + 1) + EXTRA(cc)>(); bar(); STAMP(cc + 1);
    };
    step(IC<2>{}); step(IC<3>{}); step(IC<4>{}); step(IC<5>{}); step(IC<6>{}); step(IC<7>{});
    chunk(9); vdma(1);
    waitvm<16>(); bar(); STAMP(9);
    vdma(0); kwrite(0);
    waitvm<12>(); bar(); STAMP(10);
    kwrite(1); waitlgkm0(); bar(); STAMP(11);
    if (PW == 3) {
      #pragma unroll
      for (int i = 0; i < 10; ++i) asm volatile("" :: "v"(pd[i]));
    }
    wo_blocks(0, 4);
    waitvm<5>(); STAMP(12);
    wo_blocks(4, 8);
    bar(); STAMP(13);
    wo_blocks(8, 12);
    bar(); STAMP(14);
    wo_blocks(12, 32);
    {
      const unsigned peer = (bp * 8 + ((hl + 1 + lane % 7) & 7)) * 8 + xcd;
      GAS u64* f = (GAS u64*)(ws + WS_F1 + (peer * 4 + w) * FL);
      STAMP(15);
      for (unsigned s = 0; s < SPIN_CAP; ++s) {
        const u64 v = __hip_atomic_load(f, __ATOMIC_RELAXED, __HIP_MEMORY_SCOPE_AGENT);
        if (__all(v == tag1)) break;
        __builtin_amdgcn_s_sleep(2);
      }
    }
    waitvm<0>(); STAMP(16);
    bar();
    if (hl == 0 && lane == 0) __hip_atomic_store((GAS u64*)(ws + WS_N1 + (((br * 4 + hg) * 4 + bp) * 4 + w) * FL), tag1, __ATOMIC_RELAXED, __HIP_MEMORY_SCOPE_AGENT);
  } else {
    const unsigned f8 = qq | ((g & 1) << 2);
    unsigned aA[8], aB[2];
    #pragma unroll
    for (int i = 0; i < 8; ++i) aA[i] = L0 + (8 * g + qq) * 256 + (((2 * i + (p >> 1)) ^ (f8 << 1)) << 4) + 8 * (p & 1);
    #pragma unroll
    for (int s = 0; s < 2; ++s) aB[s] = L0 + 16384 + (w * 48 + c) * 128 + (((4 * s + g) ^ ((c >> 1) & 7)) << 4);
    f32x4 acc[8][3];
    #pragma unroll
    for (int i = 0; i < 8; ++i)
      #pragma unroll
      for (int t = 0; t < 3; ++t) acc[i][t] = f32x4{0, 0, 0, 0};
    u32x4 A0[8], B0[3], A1[8], B1[3];
    auto rdA = [&](u32x4* A, unsigned sb, int s) {
      #pragma unroll
      for (int i = 0; i < 8; ++i) A[i] = cat(trread(aA[i] + sb + s * 8192), trread(aA[i] + sb + s * 8192 + 1024));
    };
    auto rdB = [&](u32x4* B, unsigned sb, int s) {
      #pragma unroll
      for (int t = 0; t < 3; ++t) B[t] = lds128(aB[s] + sb + t * 2048);
    };
    auto mm = [&](u32x4* A, u32x4* B) {
      #pragma unroll
      for (int i = 0; i < 8; ++i)
        #pragma unroll
        for (int t = 0; t < 3; ++t) acc[i][t] = mfma(A[i], B[t], acc[i][t]);
    };
    auto interleave = [&]() {
      #pragma unroll
      for (int i = 0; i < 19; ++i) { __builtin_amdgcn_sched_group_barrier(0x8, 1, 0); __builtin_amdgcn_sched_group_barrier(0x100, 1, 0); }
      __builtin_amdgcn_sched_group_barrier(0x8, 5, 0);
    };
    u64 tag1, tag2;
    asm volatile("s_load_dwordx2 %0, %2, 0x0 glc\n\ts_load_dwordx2 %1, %3, 0x0 glc\n\ts_waitcnt lgkmcnt(0)" : "=&s"(tag1), "=&s"(tag2) : "s"(pn1), "s"(pn2) : "memory");
    tag1 += 1; tag2 += 1;
    bar(); STAMP(1);
    rdB(B0, 0, 0); SB0; rdA(A0, 0, 0); SB0;
    unsigned sb = 0;
    for (int cc = 0; cc < 10; ++cc) {
      if (ABL == 1) { bar(); continue; }
      rdB(B1, sb, 1); rdA(A1, sb, 1);
      mm(A0, B0);
      interleave();
      SB0; waitlgkm0(); bar(); SB0;
      sb = sb == 0 ? 81920u : (sb == 81920u ? 40960u : 0u);
      if (cc < 9) { rdB(B0, sb, 0); rdA(A0, sb, 0); }
      mm(A1, B1);
      interleave();
    }
    STAMP(11);
    u32x4 qf[4][3];
    #pragma unroll
    for (int s = 0; s < 4; ++s)
      #pragma unroll
      for (int t = 0; t < 3; ++t) qf[s][t] = pack2(acc[2 * s][t], acc[2 * s + 1][t]);
    const unsigned Kb = L0 + ((w >> 1) ? L_K1 : L_K0) + c * 256 + 8 * (g & 1);
    f32x4 sc[6][3];
    #pragma unroll
    for (int kt = 0; kt < 6; ++kt) {
      #pragma unroll
      for (int t = 0; t < 3; ++t) sc[kt][t] = f32x4{0, 0, 0, 0};
      #pragma unroll
      for (int s = 0; s < 4; ++s) {
        const u32x4 af = cat(lds64(Kb + kt * 4096 + (((4 * s + (g >> 1)) ^ c) << 4)), lds64(Kb + kt * 4096 + (((4 * s + 2 + (g >> 1)) ^ c) << 4)));
        #pragma unroll
        for (int t = 0; t < 3; ++t) sc[kt][t] = mfma(af, qf[s][t], sc[kt][t]);
      }
    }
    const float SC = 0.08838834764831845f * 1.4426950408889634f;
    float inv[3];
    u32x4 pf[3][3];
    #pragma unroll
    for (int t = 0; t < 3; ++t) {
      float m = sc[0][t][0];
      #pragma unroll
      for (int kt = 0; kt < 6; ++kt)
        #pragma unroll
        for (int r = 0; r < 4; ++r) m = fmaxf(m, sc[kt][t][r]);
      m = xmax32(xmax16(m));
      const float ms = m * SC; float sum = 0;
      #pragma unroll
      for (int kt = 0; kt < 6; ++kt)
        #pragma unroll
        for (int r = 0; r < 4; ++r) { const float e = __builtin_amdgcn_exp2f(sc[kt][t][r] * SC - ms); sc[kt][t][r] = e; sum += e; }
      sum = xadd32(xadd16(sum));
      inv[t] = __builtin_amdgcn_rcpf(sum);
      #pragma unroll
      for (int s = 0; s < 3; ++s) pf[s][t] = pack2(sc[2 * s][t], sc[2 * s + 1][t]);
    }
    SB0; STAMP(13);
    waitlgkm0(); bar(); SB0;
    STAMP(14);
    const unsigned Vb = L0 + ((w >> 1) ? L_V1 : L_V0) + (4 * g + qq) * 256 + 8 * (p & 1);
    u32x4 of[4][3];
    {
      f32x4 o[8][3];
      #pragma unroll
      for (int i = 0; i < 8; ++i) {
        #pragma unroll
        for (int t = 0; t < 3; ++t) o[i][t] = f32x4{0, 0, 0, 0};
        const unsigned vb = Vb + (((2 * i + (p >> 1)) ^ (f8 << 1)) << 4);
        #pragma unroll
        for (int s = 0; s < 3; ++s) {
          const u32x4 af = cat(trread(vb + s * 8192), trread(vb + s * 8192 + 4096));
          #pragma unroll
          for (int t = 0; t < 3; ++t) o[i][t] = mfma(af, pf[s][t], o[i][t]);
        }
      }
      #pragma unroll
      for (int s = 0; s < 4; ++s)
        #pragma unroll
        for (int t = 0; t < 3; ++t) of[s][t] = pack2(o[2 * s][t] * inv[t], o[2 * s + 1][t] * inv[t]);
    }
    const rsrc_t rO = mkrsrc(ws + WS_O + (size_t)((br * 4 + bp) * 4 + w) * 32 * 12288, 32 * 12288);
    #pragma unroll
    for (int s = 0; s < 4; ++s)
      #pragma unroll
      for (int t = 0; t < 3; ++t) __builtin_amdgcn_raw_buffer_store_b128(of[s][t], rO, lane * 16 + (s * 3 + t) * 1024, h * 12288, 16);
    SB0; bar(); SB0;
    STAMP(15);
    waitvm<0>();
    if (lane == 0) __hip_atomic_store((GAS u64*)(ws + WS_F1 + (blockIdx.x * 4 + w) * FL), tag1, __ATOMIC_RELAXED, __HIP_MEMORY_SCOPE_AGENT);
    STAMP(16);
    bar();
    STAMP(17);
    f32x4 oc[5][3];
    #pragma unroll
    for (int m = 0; m < 5; ++m)
      #pragma unroll
      for (int t = 0; t < 3; ++t) oc[m][t] = f32x4{0, 0, 0, 0};
    constexpr int RING = RINGD;
    u32x4 bq[RING][3];
    const unsigned aW = L0 + (4 * g + qq) * 160 + 8 * p;
    const unsigned vl = lane * 16;
    auto bload = [&](int o) {
      const unsigned so = (hg * 8 + ((hl + (E2 == 1 ? 1 : (o >> 2))) & 7)) * 12288 + (o & 3) * 3072;
      if (E2 == 2) { bq[o % RING][0] = of[o & 3][0]; bq[o % RING][1] = of[o & 3][1]; bq[o % RING][2] = of[o & 3][2]; return; }
      asm volatile("buffer_load_dwordx4 %0, %1, %2, %3 offen sc1" : "=v"(bq[o % RING][0]) : "v"(vl), "s"(rO), "s"(so) : "memory");
      asm volatile("buffer_load_dwordx4 %0, %1, %2, %3 offen offset:1024 sc1" : "=v"(bq[o % RING][1]) : "v"(vl), "s"(rO), "s"(so) : "memory");
      asm volatile("buffer_load_dwordx4 %0, %1, %2, %3 offen offset:2048 sc1" : "=v"(bq[o % RING][2]) : "v"(vl), "s"(rO), "s"(so) : "memory");
    };
    u32x4 W[2][5];
    auto rdW = [&](int o) {
      #pragma unroll
      for (int m = 0; m < 5; ++m) {
        const unsigned ad = aW + (o >= 12 ? (o >= 24 ? 24 : 12) : 0) * 5120;
        const unsigned off = (o - (o >= 12 ? (o >= 24 ? 24 : 12) : 0)) * 5120 + 32 * m;
        W[o & 1][m] = cat(trread(ad + off), trread(ad + off + 16 * 160));
      }
    };
    auto kstep = [&](int o, const u32x4* B) {
      if (o < 31) rdW(o + 1);
      #pragma unroll
      for (int m = 0; m < 5; ++m)
        #pragma unroll
        for (int t = 0; t < 3; ++t) oc[m][t] = mfma(W[o & 1][m], B[t], oc[m][t]);
      #pragma unroll
      for (int i = 0; i < 10; ++i) { __builtin_amdgcn_sched_group_barrier(0x8, 1, 0); __builtin_amdgcn_sched_group_barrier(0x100, 1, 0); }
      __builtin_amdgcn_sched_group_barrier(0x8, 5, 0);
      SB0;
    };
    rdW(0);
    #pragma unroll
    for (int o = 4; o < 4 + RING - 1; ++o) bload(o);
    SB0;
    #pragma unroll
    for (int o = 0; o < 4; ++o) kstep(o, of[o]);
    auto pstep = [&](auto O) {
      constexpr int o = O.value;
      if (o + RING - 1 < 32) bload(o + RING - 1);
      constexpr int later = (o + RING - 1 < 32 ? RING - 1 : 31 - o) * 3;
      asm volatile("s_waitcnt vmcnt(%3)" : "+v"(bq[o % RING][0]), "+v"(bq[o % RING][1]), "+v"(bq[o % RING][2]) : "i"(later) : "memory");
      SB0;
      kstep(o, bq[o % RING]);
    };
    pstep(IC<4>{}); pstep(IC<5>{}); pstep(IC<6>{}); pstep(IC<7>{}); pstep(IC<8>{}); pstep(IC<9>{}); pstep(IC<10>{}); pstep(IC<11>{});
    pstep(IC<12>{}); pstep(IC<13>{}); pstep(IC<14>{}); pstep(IC<15>{}); pstep(IC<16>{}); pstep(IC<17>{}); pstep(IC<18>{}); pstep(IC<19>{});
    pstep(IC<20>{}); pstep(IC<21>{}); pstep(IC<22>{}); pstep(IC<23>{}); pstep(IC<24>{}); pstep(IC<25>{}); pstep(IC<26>{}); pstep(IC<27>{});
    pstep(IC<28>{}); pstep(IC<29>{}); pstep(IC<30>{}); pstep(IC<31>{});
    STAMP(18);
    #pragma unroll
    for (int t = 0; t < 3; ++t)
      #pragma unroll
      for (int m = 0; m < 4; m += 2)
        #pragma unroll
        for (int r = 0; r < 4; ++r) {
          auto x = __builtin_amdgcn_permlane16_swap(__float_as_uint(oc[m][t][r]), __float_as_uint(oc[m + 1][t][r]), false, false);
          oc[m][t][r] = __uint_as_float(x[0]); oc[m + 1][t][r] = __uint_as_float(x[1]);
        }
    f32x4 e4[3];
    #pragma unroll
    for (int t = 0; t < 3; ++t)
      #pragma unroll
      for (int r = 0; r < 4; ++r) {
        auto x = __builtin_amdgcn_permlane16_swap(__float_as_uint(oc[4][t][r]), __float_as_uint(oc[4][t][r]), false, false);
        oc[4][t][r] = __uint_as_float(x[0]); e4[t][r] = __uint_as_float(x[1]);
      }
    const rsrc_t rP = mkrsrc(ws + WS_P + (size_t)((((br * 4 + bp) * 8 + ns) * 4 + w) * 3) * 4 * 2560, 3 * 4 * 2560);
    const unsigned v4 = (g & 1) ? 0x80000000u : 2048 + ((g >> 1) * 16 + c) * 16;
    const unsigned tme = (hg + 3 - w) & 3;
    #pragma unroll
    for (int t = 0; t < 3; ++t) {
      if (tme != (unsigned)t) {
        const unsigned so = (t * 4 + hg) * 2560;
        __builtin_amdgcn_raw_buffer_store_b128(pack2(oc[0][t], oc[1][t]), rP, lane * 16, so, 16);
        __builtin_amdgcn_raw_buffer_store_b128(pack2(oc[2][t], oc[3][t]), rP, lane * 16 + 1024, so, 16);
        __builtin_amdgcn_raw_buffer_store_b128(pack2(oc[4][t], e4[t]), rP, v4, so, 16);
      }
    }
    waitvm<0>();
    if (lane == 0) __hip_atomic_store((GAS u64*)(ws + WS_F2 + (blockIdx.x * 4 + w) * FL), tag2, __ATOMIC_RELAXED, __HIP_MEMORY_SCOPE_AGENT);
    STAMP(19);
    if (tme < 3) {
      const unsigned peer = idx * 8 + br * 4 + ((hg + 1 + lane % 3) & 3);
      GAS u64* f = (GAS u64*)(ws + WS_F2 + (peer * 4 + w) * FL);
      for (unsigned s = 0; s < SPIN_CAP; ++s) {
        const u64 v = __hip_atomic_load(f, __ATOMIC_RELAXED, __HIP_MEMORY_SCOPE_AGENT);
        if (__all(v == tag2)) break;
        __builtin_amdgcn_s_sleep(1);
      }
      asm volatile("" ::: "memory");
      STAMP(20);
      u32x4 in[3][3];
      #pragma unroll
      for (int j = 0; j < 3; ++j) {
        const unsigned so = (tme * 4 + ((hg + 1 + j) & 3)) * 2560;
        in[j][0] = __builtin_amdgcn_raw_buffer_load_b128(rP, lane * 16, so, 16);
        in[j][1] = __builtin_amdgcn_raw_buffer_load_b128(rP, lane * 16 + 1024, so, 16);
        in[j][2] = __builtin_amdgcn_raw_buffer_load_b128(rP, v4, so, 16);
      }
      f32x4 y[6];
      #pragma unroll
      for (int m = 0; m < 5; ++m) y[m] = tme == 0 ? oc[m][0] : (tme == 1 ? oc[m][1] : oc[m][2]);
      y[5] = tme == 0 ? e4[0] : (tme == 1 ? e4[1] : e4[2]);
      #pragma unroll
      for (int j = 0; j < 3; ++j)
        #pragma unroll
        for (int u = 0; u < 3; ++u)
          #pragma unroll
          for (int d = 0; d < 4; ++d) {
            y[2 * u + (d >> 1)][2 * (d & 1)] += __uint_as_float(in[j][u][d] << 16);
            y[2 * u + (d >> 1)][2 * (d & 1) + 1] += __uint_as_float(in[j][u][d] & 0xffff0000u);
          }
      const rsrc_t rY = mkrsrc(a.out + (size_t)(bp * 192 + w * 48 + tme * 16) * 2560 + br * 1280 + ns * 160, 16 * 2560);
      const unsigned vy = c * 2560 + (g & 1) * 32 + (g >> 1) * 16;
      __builtin_amdgcn_raw_buffer_store_b128(pack2(y[0], y[1]), rY, vy, 0, 0);
      __builtin_amdgcn_raw_buffer_store_b128(pack2(y[2], y[3]), rY, vy + 64, 0, 0);
      __builtin_amdgcn_raw_buffer_store_b128(pack2(y[4], y[5]), rY, (g & 1) ? 0x80000000u : c * 2560 + 128 + (g >> 1) * 16, 0, 0);
      if (tme == 0 && lane == 0) __hip_atomic_store((GAS u64*)(ws + WS_N2 + (((br * 4 + bp) * 8 + ns) * 4 + w) * FL), tag2, __ATOMIC_RELAXED, __HIP_MEMORY_SCOPE_AGENT);
    }
    STAMP(21);
  }
}

extern "C" void kernel_launch(void* const* d_in, const int* in_sizes, int n_in,
                              void* d_out, int out_size, void* d_ws, size_t ws_size,
                              hipStream_t stream) {
  Args a;
  for (int i = 0; i < 2; ++i) {
    a.x[i] = (const char*)d_in[5 * i + 0]; a.k[i] = (const char*)d_in[5 * i + 1]; a.v[i] = (const char*)d_in[5 * i + 2];
    a.wq[i] = (const char*)d_in[5 * i + 3]; a.wo[i] = (const char*)d_in[5 * i + 4];
  }
  a.ws = (char*)d_ws; a.out = (char*)d_out;
  (void)hipFuncSetAttribute((const void*)fused, hipFuncAttributeMaxDynamicSharedMemorySize, 163840);
  fused<<<256, 512, 163840, stream>>>(a);
}
```

```cpp
#define SPI 0
#include <hip/hip_runtime.h>
#include <hip/hip_bf16.h>
typedef unsigned u32x4 __attribute__((ext_vector_type(4)));
typedef unsigned u32x2 __attribute__((ext_vector_type(2)));
typedef short s16x4 __attribute__((ext_vector_type(4)));
typedef __bf16 bf16x8 __attribute__((ext_vector_type(8)));
typedef __bf16 bf16x2 __attribute__((ext_vector_type(2)));
typedef float f32x4 __attribute__((ext_vector_type(4)));
typedef float f32x2 __attribute__((ext_vector_type(2)));
typedef unsigned long long u64;
#define DI __device__ __forceinline__
#define LAS __attribute__((address_space(3)))
#define GAS __attribute__((address_space(1)))
#define SB0 __builtin_amdgcn_sched_barrier(0)
typedef __amdgpu_buffer_rsrc_t rsrc_t;
DI unsigned rfl(unsigned x) { return __builtin_amdgcn_readfirstlane(x); }
DI f32x4 mfma(u32x4 a, u32x4 b, f32x4 c) { return __builtin_amdgcn_mfma_f32_16x16x32_bf16(__builtin_bit_cast(bf16x8, a), __builtin_bit_cast(bf16x8, b), c, 0, 0, 0); }
DI unsigned cvtpk(float lo, float hi) { f32x2 v = {lo, hi}; bf16x2 b = __builtin_convertvector(v, bf16x2); return __builtin_bit_cast(unsigned, b); }
DI u32x2 trread(unsigned a) { return __builtin_bit_cast(u32x2, __builtin_amdgcn_ds_read_tr16_b64_v4i16((LAS s16x4*)a)); }
DI u32x4 cat(u32x2 a, u32x2 b) { u32x4 r = {a[0], a[1], b[0], b[1]}; return r; }
DI u32x4 pack2(f32x4 a, f32x4 b) { u32x4 r = {cvtpk(a[0], a[1]), cvtpk(a[2], a[3]), cvtpk(b[0], b[1]), cvtpk(b[2], b[3])}; return r; }
DI u32x4 lds128(unsigned a) { return *(LAS u32x4*)a; }
DI u32x2 lds64(unsigned a) { return *(LAS u32x2*)a; }
DI rsrc_t mkrsrc(const void* p, unsigned n) { return __builtin_amdgcn_make_buffer_rsrc((void*)p, 0, n, 0x20000); }
template<int N> DI void waitvm() { asm volatile("s_waitcnt vmcnt(%0)" :: "i"(N) : "memory"); }
DI void waitlgkm0() { asm volatile("s_waitcnt lgkmcnt(0)" ::: "memory"); }
DI void bar() { asm volatile("s_barrier" ::: "memory"); }
#define ABL 0
template<int NT> DI void dma(unsigned m0v, unsigned voff, rsrc_t r, unsigned soff) {
  if (ABL == 2) return;
  if (NT) asm volatile("s_mov_b32 m0, %0\n\ts_nop 0\n\tbuffer_load_dwordx4 %1, %2, %3 offen nt lds" :: "s"(m0v), "v"(voff), "s"(r), "s"(soff) : "memory");
  else    asm volatile("s_mov_b32 m0, %0\n\ts_nop 0\n\tbuffer_load_dwordx4 %1, %2, %3 offen lds" :: "s"(m0v), "v"(voff), "s"(r), "s"(soff) : "memory");
}
#define PK 0
DI u32x4 ldnt(unsigned voff, rsrc_t r, unsigned soff) { u32x4 v;
  if (PK) asm volatile("buffer_load_dwordx4 %0, %1, %2, %3 offen nt" : "=v"(v) : "v"(voff), "s"(r), "s"(soff) : "memory");
  else asm volatile("buffer_load_dwordx4 %0, %1, %2, %3 offen" : "=v"(v) : "v"(voff), "s"(r), "s"(soff) : "memory");
  return v; }
DI float xmax16(float v) { auto r = __builtin_amdgcn_permlane16_swap(__float_as_uint(v), __float_as_uint(v), false, false); return fmaxf(__uint_as_float(r[0]), __uint_as_float(r[1])); }
DI float xmax32(float v) { auto r = __builtin_amdgcn_permlane32_swap(__float_as_uint(v), __float_as_uint(v), false, false); return fmaxf(__uint_as_float(r[0]), __uint_as_float(r[1])); }
DI float xadd16(float v) { auto r = __builtin_amdgcn_permlane16_swap(__float_as_uint(v), __float_as_uint(v), false, false); return __uint_as_float(r[0]) + __uint_as_float(r[1]); }
DI float xadd32(float v) { auto r = __builtin_amdgcn_permlane32_swap(__float_as_uint(v), __float_as_uint(v), false, false); return __uint_as_float(r[0]) + __uint_as_float(r[1]); }

#define STAMP(i) do { if ((i) == 0 || (i) == SPI) { u64 t_; asm volatile("s_memrealtime %0\n\ts_waitcnt lgkmcnt(0)" : "=s"(t_) :: "memory"); if ((i) == 0) sp0 = t_; if ((i) == SPI) sp1 = t_; } } while (0)

constexpr unsigned WS_O = 0;
constexpr unsigned WS_P = 12582912;
constexpr unsigned WS_F = WS_P + 2 * 4 * 8 * 4 * 3 * 4 * 2560;
constexpr unsigned FL = 128;
constexpr unsigned WS_F1 = WS_F;
constexpr unsigned WS_F2 = WS_F1 + 1024 * FL;
constexpr unsigned WS_N1 = WS_F2 + 1024 * FL;
constexpr unsigned WS_N2 = WS_N1 + 128 * FL;
constexpr unsigned WS_END = WS_N2 + 256 * FL;
constexpr unsigned WS_ST = 32u << 20;

constexpr unsigned L_K1 = 65536, L_K0 = 90112, L_V0 = 114688, L_V1 = 139264;
__host__ __device__ constexpr unsigned slot_of(int c) { return c % 3 == 0 ? 0u : (c % 3 == 1 ? 81920u : 40960u); }
constexpr unsigned SPIN_CAP = 1u << 17;

struct Args { const char* x[2]; const char* k[2]; const char* v[2]; const char* wq[2]; const char* wo[2]; char* ws; char* out; };

#define ROT 1
#define PW 0
#define PX 0
#define PO 0
#define FT 4
#define PR(cc) ((PW == 3 && (cc) >= 1 && (cc) <= FT) ? 1 : 0)
#define PRO 0
#define NC 2
#define RINGD 4
#define E2 0
#define KA 4
#define KB 7

template<int C> struct IC { static constexpr int value = C; };

__global__ __launch_bounds__(512, 2) void fused(Args a) {
  extern __shared__ __attribute__((aligned(16))) char smem[];
  const unsigned tid = threadIdx.x, lane = tid & 63, wv = rfl(tid >> 6), w = wv & 3;
  const unsigned xcd = blockIdx.x & 7, idx = blockIdx.x >> 3;
  const unsigned br = xcd >> 2, hg = xcd & 3, hl = idx & 7, h = hg * 8 + hl, bp = idx >> 3;
  const unsigned ns = (hl + 8 - 2 * bp) & 7;
  const unsigned c = lane & 15, g = lane >> 4, qq = c >> 2, p = lane & 3;
  const unsigned L0 = (unsigned)(size_t)(LAS char*)smem;
  char* ws = a.ws;
  u64 sp0 = 0, sp1 = 0;
  STAMP(0);
  const char* pn1 = ws + WS_N1 + (((br * 4 + hg) * 4 + bp) * 4 + w) * FL;
  const char* pn2 = ws + WS_N2 + (((br * 4 + bp) * 8 + ns) * 4 + w) * FL;
  if (wv >= 4) {
    const rsrc_t rx = mkrsrc(a.x[br], 768 * 640 * 2), rw = mkrsrc(a.wq[br], 640 * 4096 * 2);
    const rsrc_t rk = mkrsrc(a.k[br], 8 * 32 * 96 * 128 * 2), rv = mkrsrc(a.v[br], 8 * 32 * 96 * 128 * 2), ro = mkrsrc(a.wo[br], 640 * 4096 * 2);
    const unsigned rot = ROT == 0 ? 0 : (ROT == 1 ? hl + ((5 * bp) >> 1) : (ROT == 2 ? ((5 * bp) >> 1) : (ROT == 3 ? hl : (ROT == 4 ? ((bp + hl) & 1) * 5 : ((5 * ((bp + hl) & 3)) >> 1)))));
    unsigned vx[2], vw[2], vv[2];
    #pragma unroll
    for (int i = 0; i < 2; ++i) {
      vx[i] = (lane >> 3) * 1280 + (((lane & 7) ^ ((4 * i + (lane >> 4)) & 7)) << 4);
      vw[i] = g * 8192 + h * 256 + ((c ^ ((g | (i << 2)) << 1)) << 4);
      vv[i] = g * 256 + ((c ^ ((g | (i << 2)) << 1)) << 4);
    }
    const unsigned sx = (bp * 192 + 48 * w) * 1280, sw = 16 * w * 8192;
    const unsigned skv0 = ((bp * 2) * 32 + h) * 24576 + w * 6144, skv1 = skv0 + 32 * 24576;
    unsigned pd[10];
    #pragma unroll
    for (int i = 0; i < 10; ++i) pd[i] = 0;
    auto chunk = [&](int cc) {
      unsigned kc;
      if (NC == 0) { kc = cc + rot; kc = kc >= 20 ? kc - 20 : (kc >= 10 ? kc - 10 : kc); }
      else if (cc < NC) kc = cc;
      else { kc = cc + (((10 - NC) * ((bp + hl) & 3)) >> 2); kc = kc >= 10 ? kc - (10 - NC) : kc; }
      const unsigned sb = L0 + slot_of(cc);
      #pragma unroll
      for (int i = 0; i < 4; ++i) {
        if (PW == 1 || (PW >= 2 && cc < FT)) dma<1>(sb + w * 4096 + i * 1024, vw[i >> 1], rw, sw + (kc * 64 + 4 * i) * 8192);
        else dma<0>(sb + w * 4096 + i * 1024, vw[i >> 1], rw, sw + (kc * 64 + 4 * i) * 8192);
      }
      if (PW == 3 && cc >= 1 && cc <= FT) {
        unsigned kp = cc - 1 + rot; kp = kp >= 20 ? kp - 20 : (kp >= 10 ? kp - 10 : kp);
        asm volatile("buffer_load_dword %0, %1, %2, %3 offen sc1" : "=v"(pd[cc]) : "v"(lane < 32 ? (lane >> 1) * 8192 + h * 256 + (lane & 1) * 128 : 0x80000000u), "s"(rw), "s"(sw + kp * 64 * 8192) : "memory");
      }
      #pragma unroll
      for (int i = 0; i < 6; ++i) dma<PX>(sb + 16384 + w * 6144 + i * 1024, vx[i & 1], rx, sx + i * 8 * 1280 + kc * 128);
    };
    u32x4 kr[2][6];
    auto kload = [&](int b) {
      #pragma unroll
      for (int i = 0; i < 6; ++i) kr[b][i] = ldnt(lane * 16, rk, (b ? skv1 : skv0) + i * 1024);
    };
    auto kwrite = [&](int b) {
      #pragma unroll
      for (int i = 0; i < 6; ++i) {
        asm volatile("" : "+v"(kr[b][i]));
        const unsigned row = 24 * w + 4 * i + g;
        *(LAS u32x4*)(L0 + (b ? L_K1 : L_K0) + row * 256 + ((c ^ (row & 15)) << 4)) = kr[b][i];
      }
    };
    auto vdma = [&](int b) {
      #pragma unroll
      for (int i = 0; i < 6; ++i) dma<PK>(L0 + (b ? L_V1 : L_V0) + w * 6144 + i * 1024, vv[i & 1], rv, (b ? skv1 : skv0) + i * 1024);
    };
    unsigned vo[5];
    #pragma unroll
    for (int i = 0; i < 5; ++i) { const unsigned n = lane + 64 * i; vo[i] = (n / 10) * 1280 + (n % 10) * 16 + ns * 160; }
    auto wo_blocks = [&](int o0, int o1) {
      #pragma unroll
      for (int o = o0; o < o1; o += 4) {
        const unsigned oo = o + w;
        if (oo < (unsigned)o1) {
          const unsigned hh = (hl + (oo >> 2)) & 7;
          const unsigned so = ((hg * 8 + hh) * 128 + (oo & 3) * 32) * 1280;
          #pragma unroll
          for (int i = 0; i < 5; ++i) dma<PO>(L0 + oo * 5120 + i * 1024, vo[i], ro, so);
        }
      }
    };
    chunk(0); chunk(1);
    #define EXTRA(cc) (((KA >= (cc) && KA <= (cc) + 1) ? 6 : 0) + ((KB >= (cc) && KB <= (cc) + 1) ? 6 : 0))
    u64 tag1;
    asm volatile("s_load_dwordx2 %0, %1, 0x0 glc\n\ts_waitcnt vmcnt(%2)\n\ts_waitcnt lgkmcnt(0)" : "=&s"(tag1) : "s"(pn1), "i"(10 + PR(1)) : "memory");
    tag1 += 1;
    bar(); STAMP(1);
    chunk(2); if (KA == 2) kload(0);
    waitvm<10 + PR(2) + (KA == 2 ? 6 : 0)>(); bar(); STAMP(2);
    auto step = [&](auto C) {
      constexpr int cc = C.value;
      chunk(cc + 1);
      if (KA == cc + 1) kload(0);
      if (KB == cc + 1) kload(1);
      waitvm<10 + PR(cc + 1) + EXTRA(cc)>(); bar(); STAMP(cc + 1);
    };
    step(IC<2>{}); step(IC<3>{}); step(IC<4>{}); step(IC<5>{}); step(IC<6>{}); step(IC<7>{});
    chunk(9); vdma(1);
    waitvm<16>(); bar(); STAMP(9);
    vdma(0); kwrite(0);
    waitvm<12>(); bar(); STAMP(10);
    kwrite(1); waitlgkm0(); bar(); STAMP(11);
    if (PW == 3) {
      #pragma unroll
      for (int i = 0; i < 10; ++i) asm volatile("" :: "v"(pd[i]));
    }
    wo_blocks(0, 4);
    waitvm<5>(); STAMP(12);
    wo_blocks(4, 8);
    bar(); STAMP(13);
    wo_blocks(8, 12);
    bar(); STAMP(14);
    wo_blocks(12, 32);
    {
      const unsigned peer = (bp * 8 + ((hl + 1 + lane % 7) & 7)) * 8 + xcd;
      GAS u64* f = (GAS u64*)(ws + WS_F1 + (peer * 4 + w) * FL);
      STAMP(15);
      for (unsigned s = 0; s < SPIN_CAP; ++s) {
        const u64 v = __hip_atomic_load(f, __ATOMIC_RELAXED, __HIP_MEMORY_SCOPE_AGENT);
        if (__all(v == tag1)) break;
        __builtin_amdgcn_s_sleep(2);
      }
    }
    waitvm<0>(); STAMP(16);
    bar();
    if (hl == 0 && lane == 0) __hip_atomic_store((GAS u64*)(ws + WS_N1 + (((br * 4 + hg) * 4 + bp) * 4 + w) * FL), tag1, __ATOMIC_RELAXED, __HIP_MEMORY_SCOPE_AGENT);
  } else {
    const unsigned f8 = qq | ((g & 1) << 2);
    unsigned aA[8], aB[2];
    #pragma unroll
    for (int i = 0; i < 8; ++i) aA[i] = L0 + (8 * g + qq) * 256 + (((2 * i + (p >> 1)) ^ (f8 << 1)) << 4) + 8 * (p & 1);
    #pragma unroll
    for (int s = 0; s < 2; ++s) aB[s] = L0 + 16384 + (w * 48 + c) * 128 + (((4 * s + g) ^ ((c >> 1) & 7)) << 4);
    f32x4 acc[8][3];
    #pragma unroll
    for (int i = 0; i < 8; ++i)
      #pragma unroll
      for (int t = 0; t < 3; ++t) acc[i][t] = f32x4{0, 0, 0, 0};
    u32x4 A0[8], B0[3], A1[8], B1[3];
    auto rdA = [&](u32x4* A, unsigned sb, int s) {
      #pragma unroll
      for (int i = 0; i < 8; ++i) A[i] = cat(trread(aA[i] + sb + s * 8192), trread(aA[i] + sb + s * 8192 + 1024));
    };
    auto rdB = [&](u32x4* B, unsigned sb, int s) {
      #pragma unroll
      for (int t = 0; t < 3; ++t) B[t] = lds128(aB[s] + sb + t * 2048);
    };
    auto mm = [&](u32x4* A, u32x4* B) {
      #pragma unroll
      for (int i = 0; i < 8; ++i)
        #pragma unroll
        for (int t = 0; t < 3; ++t) acc[i][t] = mfma(A[i], B[t], acc[i][t]);
    };
    auto interleave = [&]() {
      #pragma unroll
      for (int i = 0; i < 19; ++i) { __builtin_amdgcn_sched_group_barrier(0x8, 1, 0); __builtin_amdgcn_sched_group_barrier(0x100, 1, 0); }
      __builtin_amdgcn_sched_group_barrier(0x8, 5, 0);
    };
    u64 tag1, tag2;
    asm volatile("s_load_dwordx2 %0, %2, 0x0 glc\n\ts_load_dwordx2 %1, %3, 0x0 glc\n\ts_waitcnt lgkmcnt(0)" : "=&s"(tag1), "=&s"(tag2) : "s"(pn1), "s"(pn2) : "memory");
    tag1 += 1; tag2 += 1;
    bar(); STAMP(1);
    rdB(B0, 0, 0); SB0; rdA(A0, 0, 0); SB0;
    unsigned sb = 0;
    for (int cc = 0; cc < 10; ++cc) {
      if (ABL == 1) { bar(); continue; }
      rdB(B1, sb, 1); rdA(A1, sb, 1);
      mm(A0, B0);
      interleave();
      SB0; waitlgkm0(); bar(); SB0;
      sb = sb == 0 ? 81920u : (sb == 81920u ? 40960u : 0u);
      if (cc < 9) { rdB(B0, sb, 0); rdA(A0, sb, 0); }
      mm(A1, B1);
      interleave();
    }
    STAMP(11);
    u32x4 qf[4][3];
    #pragma unroll
    for (int s = 0; s < 4; ++s)
      #pragma unroll
      for (int t = 0; t < 3; ++t) qf[s][t] = pack2(acc[2 * s][t], acc[2 * s + 1][t]);
    const unsigned Kb = L0 + ((w >> 1) ? L_K1 : L_K0) + c * 256 + 8 * (g & 1);
    f32x4 sc[6][3];
    #pragma unroll
    for (int kt = 0; kt < 6; ++kt) {
      #pragma unroll
      for (int t = 0; t < 3; ++t) sc[kt][t] = f32x4{0, 0, 0, 0};
      #pragma unroll
      for (int s = 0; s < 4; ++s) {
        const u32x4 af = cat(lds64(Kb + kt * 4096 + (((4 * s + (g >> 1)) ^ c) << 4)), lds64(Kb + kt * 4096 + (((4 * s + 2 + (g >> 1)) ^ c) << 4)));
        #pragma unroll
        for (int t = 0; t < 3; ++t) sc[kt][t] = mfma(af, qf[s][t], sc[kt][t]);
      }
    }
    const float SC = 0.08838834764831845f * 1.4426950408889634f;
    float inv[3];
    u32x4 pf[3][3];
    #pragma unroll
    for (int t = 0; t < 3; ++t) {
      float m = sc[0][t][0];
      #pragma unroll
      for (int kt = 0; kt < 6; ++kt)
        #pragma unroll
        for (int r = 0; r < 4; ++r) m = fmaxf(m, sc[kt][t][r]);
      m = xmax32(xmax16(m));
      const float ms = m * SC; float sum = 0;
      #pragma unroll
      for (int kt = 0; kt < 6; ++kt)
        #pragma unroll
        for (int r = 0; r < 4; ++r) { const float e = __builtin_amdgcn_exp2f(sc[kt][t][r] * SC - ms); sc[kt][t][r] = e; sum += e; }
      sum = xadd32(xadd16(sum));
      inv[t] = __builtin_amdgcn_rcpf(sum);
      #pragma unroll
      for (int s = 0; s < 3; ++s) pf[s][t] = pack2(sc[2 * s][t], sc[2 * s + 1][t]);
    }
    SB0; STAMP(13);
    waitlgkm0(); bar(); SB0;
    STAMP(14);
    const unsigned Vb = L0 + ((w >> 1) ? L_V1 : L_V0) + (4 * g + qq) * 256 + 8 * (p & 1);
    u32x4 of[4][3];
    {
      f32x4 o[8][3];
      #pragma unroll
      for (int i = 0; i < 8; ++i) {
        #pragma unroll
        for (int t = 0; t < 3; ++t) o[i][t] = f32x4{0, 0, 0, 0};
        const unsigned vb = Vb + (((2 * i + (p >> 1)) ^ (f8 << 1)) << 4);
        #pragma unroll
        for (int s = 0; s < 3; ++s) {
          const u32x4 af = cat(trread(vb + s * 8192), trread(vb + s * 8192 + 4096));
          #pragma unroll
          for (int t = 0; t < 3; ++t) o[i][t] = mfma(af, pf[s][t], o[i][t]);
        }
      }
      #pragma unroll
      for (int s = 0; s < 4; ++s)
        #pragma unroll
        for (int t = 0; t < 3; ++t) of[s][t] = pack2(o[2 * s][t] * inv[t], o[2 * s + 1][t] * inv[t]);
    }
    const rsrc_t rO = mkrsrc(ws + WS_O + (size_t)((br * 4 + bp) * 4 + w) * 32 * 12288, 32 * 12288);
    #pragma unroll
    for (int s = 0; s < 4; ++s)
      #pragma unroll
      for (int t = 0; t < 3; ++t) __builtin_amdgcn_raw_buffer_store_b128(of[s][t], rO, lane * 16 + (s * 3 + t) * 1024, h * 12288, 16);
    SB0; bar(); SB0;
    STAMP(15);
    waitvm<0>();
    if (lane == 0) __hip_atomic_store((GAS u64*)(ws + WS_F1 + (blockIdx.x * 4 + w) * FL), tag1, __ATOMIC_RELAXED, __HIP_MEMORY_SCOPE_AGENT);
    STAMP(16);
    bar();
    STAMP(17);
    f32x4 oc[5][3];
    #pragma unroll
    for (int m = 0; m < 5; ++m)
      #pragma unroll
      for (int t = 0; t < 3; ++t) oc[m][t] = f32x4{0, 0, 0, 0};
    constexpr int RING = RINGD;
    u32x4 bq[RING][3];
    const unsigned aW = L0 + (4 * g + qq) * 160 + 8 * p;
    const unsigned vl = lane * 16;
    auto bload = [&](int o) {
      const unsigned so = (hg * 8 + ((hl + (E2 == 1 ? 1 : (o >> 2))) & 7)) * 12288 + (o & 3) * 3072;
      if (E2 == 2) { bq[o % RING][0] = of[o & 3][0]; bq[o % RING][1] = of[o & 3][1]; bq[o % RING][2] = of[o & 3][2]; return; }
      asm volatile("buffer_load_dwordx4 %0, %1, %2, %3 offen sc1" : "=v"(bq[o % RING][0]) : "v"(vl), "s"(rO), "s"(so) : "memory");
      asm volatile("buffer_load_dwordx4 %0, %1, %2, %3 offen offset:1024 sc1" : "=v"(bq[o % RING][1]) : "v"(vl), "s"(rO), "s"(so) : "memory");
      asm volatile("buffer_load_dwordx4 %0, %1, %2, %3 offen offset:2048 sc1" : "=v"(bq[o % RING][2]) : "v"(vl), "s"(rO), "s"(so) : "memory");
    };
    u32x4 W[2][5];
    auto rdW = [&](int o) {
      #pragma unroll
      for (int m = 0; m < 5; ++m) {
        const unsigned ad = aW + (o >= 12 ? (o >= 24 ? 24 : 12) : 0) * 5120;
        const unsigned off = (o - (o >= 12 ? (o >= 24 ? 24 : 12) : 0)) * 5120 + 32 * m;
        W[o & 1][m] = cat(trread(ad + off), trread(ad + off + 16 * 160));
      }
    };
    auto kstep = [&](int o, const u32x4* B) {
      if (o < 31) rdW(o + 1);
      #pragma unroll
      for (int m = 0; m < 5; ++m)
        #pragma unroll
        for (int t = 0; t < 3; ++t) oc[m][t] = mfma(W[o & 1][m], B[t], oc[m][t]);
      #pragma unroll
      for (int i = 0; i < 10; ++i) { __builtin_amdgcn_sched_group_barrier(0x8, 1, 0); __builtin_amdgcn_sched_group_barrier(0x100, 1, 0); }
      __builtin_amdgcn_sched_group_barrier(0x8, 5, 0);
      SB0;
    };
    rdW(0);
    #pragma unroll
    for (int o = 4; o < 4 + RING - 1; ++o) bload(o);
    SB0;
    #pragma unroll
    for (int o = 0; o < 4; ++o) kstep(o, of[o]);
    auto pstep = [&](auto O) {
      constexpr int o = O.value;
      if (o + RING - 1 < 32) bload(o + RING - 1);
      constexpr int later = (o + RING - 1 < 32 ? RING - 1 : 31 - o) * 3;
      asm volatile("s_waitcnt vmcnt(%3)" : "+v"(bq[o % RING][0]), "+v"(bq[o % RING][1]), "+v"(bq[o % RING][2]) : "i"(later) : "memory");
      SB0;
      kstep(o, bq[o % RING]);
    };
    pstep(IC<4>{}); pstep(IC<5>{}); pstep(IC<6>{}); pstep(IC<7>{}); pstep(IC<8>{}); pstep(IC<9>{}); pstep(IC<10>{}); pstep(IC<11>{});
    pstep(IC<12>{}); pstep(IC<13>{}); pstep(IC<14>{}); pstep(IC<15>{}); pstep(IC<16>{}); pstep(IC<17>{}); pstep(IC<18>{}); pstep(IC<19>{});
    pstep(IC<20>{}); pstep(IC<21>{}); pstep(IC<22>{}); pstep(IC<23>{}); pstep(IC<24>{}); pstep(IC<25>{}); pstep(IC<26>{}); pstep(IC<27>{});
    pstep(IC<28>{}); pstep(IC<29>{}); pstep(IC<30>{}); pstep(IC<31>{});
    STAMP(18);
    #pragma unroll
    for (int t = 0; t < 3; ++t)
      #pragma unroll
      for (int m = 0; m < 4; m += 2)
        #pragma unroll
        for (int r = 0; r < 4; ++r) {
          auto x = __builtin_amdgcn_permlane16_swap(__float_as_uint(oc[m][t][r]), __float_as_uint(oc[m + 1][t][r]), false, false);
          oc[m][t][r] = __uint_as_float(x[0]); oc[m + 1][t][r] = __uint_as_float(x[1]);
        }
    f32x4 e4[3];
    #pragma unroll
    for (int t = 0; t < 3; ++t)
      #pragma unroll
      for (int r = 0; r < 4; ++r) {
        auto x = __builtin_amdgcn_permlane16_swap(__float_as_uint(oc[4][t][r]), __float_as_uint(oc[4][t][r]), false, false);
        oc[4][t][r] = __uint_as_float(x[0]); e4[t][r] = __uint_as_float(x[1]);
      }
    const rsrc_t rP = mkrsrc(ws + WS_P + (size_t)((((br * 4 + bp) * 8 + ns) * 4 + w) * 3) * 4 * 2560, 3 * 4 * 2560);
    const unsigned v4 = (g & 1) ? 0x80000000u : 2048 + ((g >> 1) * 16 + c) * 16;
    const unsigned tme = (hg + 3 - w) & 3;
    #pragma unroll
    for (int t = 0; t < 3; ++t) {
      if (tme != (unsigned)t) {
        const unsigned so = (t * 4 + hg) * 2560;
        __builtin_amdgcn_raw_buffer_store_b128(pack2(oc[0][t], oc[1][t]), rP, lane * 16, so, 16);
        __builtin_amdgcn_raw_buffer_store_b128(pack2(oc[2][t], oc[3][t]), rP, lane * 16 + 1024, so, 16);
        __builtin_amdgcn_raw_buffer_store_b128(pack2(oc[4][t], e4[t]), rP, v4, so, 16);
      }
    }
    waitvm<0>();
    if (lane == 0) __hip_atomic_store((GAS u64*)(ws + WS_F2 + (blockIdx.x * 4 + w) * FL), tag2, __ATOMIC_RELAXED, __HIP_MEMORY_SCOPE_AGENT);
    STAMP(19);
    if (tme < 3) {
      const unsigned peer = idx * 8 + br * 4 + ((hg + 1 + lane % 3) & 3);
      GAS u64* f = (GAS u64*)(ws + WS_F2 + (peer * 4 + w) * FL);
      for (unsigned s = 0; s < SPIN_CAP; ++s) {
        const u64 v = __hip_atomic_load(f, __ATOMIC_RELAXED, __HIP_MEMORY_SCOPE_AGENT);
        if (__all(v == tag2)) break;
        __builtin_amdgcn_s_sleep(1);
      }
      asm volatile("" ::: "memory");
      STAMP(20);
      u32x4 in[3][3];
      #pragma unroll
      for (int j = 0; j < 3; ++j) {
        const unsigned so = (tme * 4 + ((hg + 1 + j) & 3)) * 2560;
        in[j][0] = __builtin_amdgcn_raw_buffer_load_b128(rP, lane * 16, so, 16);
        in[j][1] = __builtin_amdgcn_raw_buffer_load_b128(rP, lane * 16 + 1024, so, 16);
        in[j][2] = __builtin_amdgcn_raw_buffer_load_b128(rP, v4, so, 16);
      }
      f32x4 y[6];
      #pragma unroll
      for (int m = 0; m < 5; ++m) y[m] = tme == 0 ? oc[m][0] : (tme == 1 ? oc[m][1] : oc[m][2]);
      y[5] = tme == 0 ? e4[0] : (tme == 1 ? e4[1] : e4[2]);
      #pragma unroll
      for (int j = 0; j < 3; ++j)
        #pragma unroll
        for (int u = 0; u < 3; ++u)
          #pragma unroll
          for (int d = 0; d < 4; ++d) {
            y[2 * u + (d >> 1)][2 * (d & 1)] += __uint_as_float(in[j][u][d] << 16);
            y[2 * u + (d >> 1)][2 * (d & 1) + 1] += __uint_as_float(in[j][u][d] & 0xffff0000u);
          }
      const rsrc_t rY = mkrsrc(a.out + (size_t)(bp * 192 + w * 48 + tme * 16) * 2560 + br * 1280 + ns * 160, 16 * 2560);
      const unsigned vy = c * 2560 + (g & 1) * 32 + (g >> 1) * 16;
      __builtin_amdgcn_raw_buffer_store_b128(pack2(y[0], y[1]), rY, vy, 0, 0);
      __builtin_amdgcn_raw_buffer_store_b128(pack2(y[2], y[3]), rY, vy + 64, 0, 0);
      __builtin_amdgcn_raw_buffer_store_b128(pack2(y[4], y[5]), rY, (g & 1) ? 0x80000000u : c * 2560 + 128 + (g >> 1) * 16, 0, 0);
      if (tme == 0 && lane == 0) __hip_atomic_store((GAS u64*)(ws + WS_N2 + (((br * 4 + bp) * 8 + ns) * 4 + w) * FL), tag2, __ATOMIC_RELAXED, __HIP_MEMORY_SCOPE_AGENT);
    }
    STAMP(21);
    { const u64 tgt = sp0 + 5000 + 4 * (sp1 - sp0); while (__builtin_amdgcn_s_memrealtime() < tgt) __builtin_amdgcn_s_sleep(2); }
  }
}

extern "C" void kernel_launch(void* const* d_in, const int* in_sizes, int n_in,
                              void* d_out, int out_size, void* d_ws, size_t ws_size,
                              hipStream_t stream) {
  Args a;
  for (int i = 0; i < 2; ++i) {
    a.x[i] = (const char*)d_in[5 * i + 0]; a.k[i] = (const char*)d_in[5 * i + 1]; a.v[i] = (const char*)d_in[5 * i + 2];
    a.wq[i] = (const char*)d_in[5 * i + 3]; a.wo[i] = (const char*)d_in[5 * i + 4];
  }
  a.ws = (char*)d_ws; a.out = (char*)d_out;
  (void)hipFuncSetAttribute((const void*)fused, hipFuncAttributeMaxDynamicSharedMemorySize, 163840);
  fused<<<256, 512, 163840, stream>>>(a);
}
```

```cpp
#define SPI 0
#include <hip/hip_runtime.h>
#include <hip/hip_bf16.h>
typedef unsigned u32x4 __attribute__((ext_vector_type(4)));
typedef unsigned u32x2 __attribute__((ext_vector_type(2)));
typedef short s16x4 __attribute__((ext_vector_type(4)));
typedef __bf16 bf16x8 __attribute__((ext_vector_type(8)));
typedef __bf16 bf16x2 __attribute__((ext_vector_type(2)));
typedef float f32x4 __attribute__((ext_vector_type(4)));
typedef float f32x2 __attribute__((ext_vector_type(2)));
typedef unsigned long long u64;
#define DI __device__ __forceinline__
#define LAS __attribute__((address_space(3)))
#define GAS __attribute__((address_space(1)))
#define SB0 __builtin_amdgcn_sched_barrier(0)
typedef __amdgpu_buffer_rsrc_t rsrc_t;
DI unsigned rfl(unsigned x) { return __builtin_amdgcn_readfirstlane(x); }
DI f32x4 mfma(u32x4 a, u32x4 b, f32x4 c) { return __builtin_amdgcn_mfma_f32_16x16x32_bf16(__builtin_bit_cast(bf16x8, a), __builtin_bit_cast(bf16x8, b), c, 0, 0, 0); }
DI unsigned cvtpk(float lo, float hi) { f32x2 v = {lo, hi}; bf16x2 b = __builtin_convertvector(v, bf16x2); return __builtin_bit_cast(unsigned, b); }
DI u32x2 trread(unsigned a) { return __builtin_bit_cast(u32x2, __builtin_amdgcn_ds_read_tr16_b64_v4i16((LAS s16x4*)a)); }
DI u32x4 cat(u32x2 a, u32x2 b) { u32x4 r = {a[0], a[1], b[0], b[1]}; return r; }
DI u32x4 pack2(f32x4 a, f32x4 b) { u32x4 r = {cvtpk(a[0], a[1]), cvtpk(a[2], a[3]), cvtpk(b[0], b[1]), cvtpk(b[2], b[3])}; return r; }
DI u32x4 lds128(unsigned a) { return *(LAS u32x4*)a; }
DI u32x2 lds64(unsigned a) { return *(LAS u32x2*)a; }
DI rsrc_t mkrsrc(const void* p, unsigned n) { return __builtin_amdgcn_make_buffer_rsrc((void*)p, 0, n, 0x20000); }
template<int N> DI void waitvm() { asm volatile("s_waitcnt vmcnt(%0)" :: "i"(N) : "memory"); }
DI void waitlgkm0() { asm volatile("s_waitcnt lgkmcnt(0)" ::: "memory"); }
DI void bar() { asm volatile("s_barrier" ::: "memory"); }
#define ABL 0
template<int NT> DI void dma(unsigned m0v, unsigned voff, rsrc_t r, unsigned soff) {
  if (ABL == 2) return;
  if (NT) asm volatile("s_mov_b32 m0, %0\n\ts_nop 0\n\tbuffer_load_dwordx4 %1, %2, %3 offen nt lds" :: "s"(m0v), "v"(voff), "s"(r), "s"(soff) : "memory");
  else    asm volatile("s_mov_b32 m0, %0\n\ts_nop 0\n\tbuffer_load_dwordx4 %1, %2, %3 offen lds" :: "s"(m0v), "v"(voff), "s"(r), "s"(soff) : "memory");
}
#define PK 0
DI u32x4 ldnt(unsigned voff, rsrc_t r, unsigned soff) { u32x4 v;
  if (PK) asm volatile("buffer_load_dwordx4 %0, %1, %2, %3 offen nt" : "=v"(v) : "v"(voff), "s"(r), "s"(soff) : "memory");
  else asm volatile("buffer_load_dwordx4 %0, %1, %2, %3 offen" : "=v"(v) : "v"(voff), "s"(r), "s"(soff) : "memory");
  return v; }
DI float xmax16(float v) { auto r = __builtin_amdgcn_permlane16_swap(__float_as_uint(v), __float_as_uint(v), false, false); return fmaxf(__uint_as_float(r[0]), __uint_as_float(r[1])); }
DI float xmax32(float v) { auto r = __builtin_amdgcn_permlane32_swap(__float_as_uint(v), __float_as_uint(v), false, false); return fmaxf(__uint_as_float(r[0]), __uint_as_float(r[1])); }
DI float xadd16(float v) { auto r = __builtin_amdgcn_permlane16_swap(__float_as_uint(v), __float_as_uint(v), false, false); return __uint_as_float(r[0]) + __uint_as_float(r[1]); }
DI float xadd32(float v) { auto r = __builtin_amdgcn_permlane32_swap(__float_as_uint(v), __float_as_uint(v), false, false); return __uint_as_float(r[0]) + __uint_as_float(r[1]); }

#define STAMP(i) do { if ((i) == 0 || (i) == SPI) { u64 t_; asm volatile("s_memrealtime %0\n\ts_waitcnt lgkmcnt(0)" : "=s"(t_) :: "memory"); if ((i) == 0) sp0 = t_; if ((i) == SPI) sp1 = t_; } } while (0)

constexpr unsigned WS_O = 0;
constexpr unsigned WS_P = 12582912;
constexpr unsigned WS_F = WS_P + 2 * 4 * 8 * 4 * 3 * 4 * 2560;
constexpr unsigned FL = 128;
constexpr unsigned WS_F1 = WS_F;
constexpr unsigned WS_F2 = WS_F1 + 1024 * FL;
constexpr unsigned WS_N1 = WS_F2 + 1024 * FL;
constexpr unsigned WS_N2 = WS_N1 + 128 * FL;
constexpr unsigned WS_END = WS_N2 + 256 * FL;
constexpr unsigned WS_ST = 32u << 20;

constexpr unsigned L_K1 = 65536, L_K0 = 90112, L_V0 = 114688, L_V1 = 139264;
__host__ __device__ constexpr unsigned slot_of(int c) { return c % 3 == 0 ? 0u : (c % 3 == 1 ? 81920u : 40960u); }
constexpr unsigned SPIN_CAP = 1u << 17;

struct Args { const char* x[2]; const char* k[2]; const char* v[2]; const char* wq[2]; const char* wo[2]; char* ws; char* out; };

#define ROT 1
#define PW 0
#define PX 0
#define PO 0
#define FT 4
#define PR(cc) ((PW == 3 && (cc) >= 1 && (cc) <= FT) ? 1 : 0)
#define PRO 0
#define NC 2
#define RINGD 4
#define E2 0
#define KA 4
#define KB 7

template<int C> struct IC { static constexpr int value = C; };

__global__ __launch_bounds__(512, 2) void fused(Args a) {
  extern __shared__ __attribute__((aligned(16))) char smem[];
  const unsigned tid = threadIdx.x, lane = tid & 63, wv = rfl(tid >> 6), w = wv & 3;
  const unsigned xcd = blockIdx.x & 7, idx = blockIdx.x >> 3;
  const unsigned br = xcd >> 2, hg = xcd & 3, hl = idx & 7, h = hg * 8 + hl, bp = idx >> 3;
  const unsigned ns = (hl + 8 - 2 * bp) & 7;
  const unsigned c = lane & 15, g = lane >> 4, qq = c >> 2, p = lane & 3;
  const unsigned L0 = (unsigned)(size_t)(LAS char*)smem;
  char* ws = a.ws;
  u64 sp0 = 0, sp1 = 0;
  STAMP(0);
  const char* pn1 = ws + WS_N1 + (((br * 4 + hg) * 4 + bp) * 4 + w) * FL;
  const char* pn2 = ws + WS_N2 + (((br * 4 + bp) * 8 + ns) * 4 + w) * FL;
  if (wv >= 4) {
    const rsrc_t rx = mkrsrc(a.x[br], 768 * 640 * 2), rw = mkrsrc(a.wq[br], 640 * 4096 * 2);
    const rsrc_t rk = mkrsrc(a.k[br], 8 * 32 * 96 * 128 * 2), rv = mkrsrc(a.v[br], 8 * 32 * 96 * 128 * 2), ro = mkrsrc(a.wo[br], 640 * 4096 * 2);
    const unsigned rot = ROT == 0 ? 0 : (ROT == 1 ? hl + ((5 * bp) >> 1) : (ROT == 2 ? ((5 * bp) >> 1) : (ROT == 3 ? hl : (ROT == 4 ? ((bp + hl) & 1) * 5 : ((5 * ((bp + hl) & 3)) >> 1)))));
    unsigned vx[2], vw[2], vv[2];
    #pragma unroll
    for (int i = 0; i < 2; ++i) {
      vx[i] = (lane >> 3) * 1280 + (((lane & 7) ^ ((4 * i + (lane >> 4)) & 7)) << 4);
      vw[i] = g * 8192 + h * 256 + ((c ^ ((g | (i << 2)) << 1)) << 4);
      vv[i] = g * 256 + ((c ^ ((g | (i << 2)) << 1)) << 4);
    }
    const unsigned sx = (bp * 192 + 48 * w) * 1280, sw = 16 * w * 8192;
    const unsigned skv0 = ((bp * 2) * 32 + h) * 24576 + w * 6144, skv1 = skv0 + 32 * 24576;
    unsigned pd[10];
    #pragma unroll
    for (int i = 0; i < 10; ++i) pd[i] = 0;
    auto chunk = [&](int cc) {
      unsigned kc;
      if (NC == 0) { kc = cc + rot; kc = kc >= 20 ? kc - 20 : (kc >= 10 ? kc - 10 : kc); }
      else if (cc < NC) kc = cc;
      else { kc = cc + (((10 - NC) * ((bp + hl) & 3)) >> 2); kc = kc >= 10 ? kc - (10 - NC) : kc; }
      const unsigned sb = L0 + slot_of(cc);
      #pragma unroll
      for (int i = 0; i < 4; ++i) {
        if (PW == 1 || (PW >= 2 && cc < FT)) dma<1>(sb + w * 4096 + i * 1024, vw[i >> 1], rw, sw + (kc * 64 + 4 * i) * 8192);
        else dma<0>(sb + w * 4096 + i * 1024, vw[i >> 1], rw, sw + (kc * 64 + 4 * i) * 8192);
      }
      if (PW == 3 && cc >= 1 && cc <= FT) {
        unsigned kp = cc - 1 + rot; kp = kp >= 20 ? kp - 20 : (kp >= 10 ? kp - 10 : kp);
        asm volatile("buffer_load_dword %0, %1, %2, %3 offen sc1" : "=v"(pd[cc]) : "v"(lane < 32 ? (lane >> 1) * 8192 + h * 256 + (lane & 1) * 128 : 0x80000000u), "s"(rw), "s"(sw + kp * 64 * 8192) : "memory");
      }
      #pragma unroll
      for (int i = 0; i < 6; ++i) dma<PX>(sb + 16384 + w * 6144 + i * 1024, vx[i & 1], rx, sx + i * 8 * 1280 + kc * 128);
    };
    u32x4 kr[2][6];
    auto kload = [&](int b) {
      #pragma unroll
      for (int i = 0; i < 6; ++i) kr[b][i] = ldnt(lane * 16, rk, (b ? skv1 : skv0) + i * 1024);
    };
    auto kwrite = [&](int b) {
      #pragma unroll
      for (int i = 0; i < 6; ++i) {
        asm volatile("" : "+v"(kr[b][i]));
        const unsigned row = 24 * w + 4 * i + g;
        *(LAS u32x4*)(L0 + (b ? L_K1 : L_K0) + row * 256 + ((c ^ (row & 15)) << 4)) = kr[b][i];
      }
    };
    auto vdma = [&](int b) {
      #pragma unroll
      for (int i = 0; i < 6; ++i) dma<PK>(L0 + (b ? L_V1 : L_V0) + w * 6144 + i * 1024, vv[i & 1], rv, (b ? skv1 : skv0) + i * 1024);
    };
    unsigned vo[5];
    #pragma unroll
    for (int i = 0; i < 5; ++i) { const unsigned n = lane + 64 * i; vo[i] = (n / 10) * 1280 + (n % 10) * 16 + ns * 160; }
    auto wo_blocks = [&](int o0, int o1) {
      #pragma unroll
      for (int o = o0; o < o1; o += 4) {
        const unsigned oo = o + w;
        if (oo < (unsigned)o1) {
          const unsigned hh = (hl + (oo >> 2)) & 7;
          const unsigned so = ((hg * 8 + hh) * 128 + (oo & 3) * 32) * 1280;
          #pragma unroll
          for (int i = 0; i < 5; ++i) dma<PO>(L0 + oo * 5120 + i * 1024, vo[i], ro, so);
        }
      }
    };
    chunk(0); chunk(1);
    #define EXTRA(cc) (((KA >= (cc) && KA <= (cc) + 1) ? 6 : 0) + ((KB >= (cc) && KB <= (cc) + 1) ? 6 : 0))
    u64 tag1;
    asm volatile("s_load_dwordx2 %0, %1, 0x0 glc\n\ts_waitcnt vmcnt(%2)\n\ts_waitcnt lgkmcnt(0)" : "=&s"(tag1) : "s"(pn1), "i"(10 + PR(1)) : "memory");
    tag1 += 1;
    bar(); STAMP(1);
    chunk(2); if (KA == 2) kload(0);
    waitvm<10 + PR(2) + (KA == 2 ? 6 : 0)>(); bar(); STAMP(2);
    auto step = [&](auto C) {
      constexpr int cc = C.value;
      chunk(cc + 1);
      if (KA == cc + 1) kload(0);
      if (KB == cc + 1) kload(1);
      waitvm<10 + PR(cc + 1) + EXTRA(cc)>(); bar(); STAMP(cc + 1);
    };
    step(IC<2>{}); step(IC<3>{}); step(IC<4>{}); step(IC<5>{}); step(IC<6>{}); step(IC<7>{});
    chunk(9); vdma(1);
    waitvm<16>(); bar(); STAMP(9);
    vdma(0); kwrite(0);
    waitvm<12>(); bar(); STAMP(10);
    kwrite(1); waitlgkm0(); bar(); STAMP(11);
    if (PW == 3) {
      #pragma unroll
      for (int i = 0; i < 10; ++i) asm volatile("" :: "v"(pd[i]));
    }
    wo_blocks(0, 4);
    waitvm<5>(); STAMP(12);
    wo_blocks(4, 8);
    bar(); STAMP(13);
    wo_blocks(8, 12);
    bar(); STAMP(14);
    wo_blocks(12, 32);
    {
      const unsigned peer = (bp * 8 + ((hl + 1 + lane % 7) & 7)) * 8 + xcd;
      GAS u64* f = (GAS u64*)(ws + WS_F1 + (peer * 4 + w) * FL);
      STAMP(15);
      for (unsigned s = 0; s < SPIN_CAP; ++s) {
        const u64 v = __hip_atomic_load(f, __ATOMIC_RELAXED, __HIP_MEMORY_SCOPE_AGENT);
        if (__all(v == tag1)) break;
        __builtin_amdgcn_s_sleep(2);
      }
    }
    waitvm<0>(); STAMP(16);
    bar();
    if (hl == 0 && lane == 0) __hip_atomic_store((GAS u64*)(ws + WS_N1 + (((br * 4 + hg) * 4 + bp) * 4 + w) * FL), tag1, __ATOMIC_RELAXED, __HIP_MEMORY_SCOPE_AGENT);
  } else {
    const unsigned f8 = qq | ((g & 1) << 2);
    unsigned aA[8], aB[2];
    #pragma unroll
    for (int i = 0; i < 8; ++i) aA[i] = L0 + (8 * g + qq) * 256 + (((2 * i + (p >> 1)) ^ (f8 << 1)) << 4) + 8 * (p & 1);
    #pragma unroll
    for (int s = 0; s < 2; ++s) aB[s] = L0 + 16384 + (w * 48 + c) * 128 + (((4 * s + g) ^ ((c >> 1) & 7)) << 4);
    f32x4 acc[8][3];
    #pragma unroll
    for (int i = 0; i < 8; ++i)
      #pragma unroll
      for (int t = 0; t < 3; ++t) acc[i][t] = f32x4{0, 0, 0, 0};
    u32x4 A0[8], B0[3], A1[8], B1[3];
    auto rdA = [&](u32x4* A, unsigned sb, int s) {
      #pragma unroll
      for (int i = 0; i < 8; ++i) A[i] = cat(trread(aA[i] + sb + s * 8192), trread(aA[i] + sb + s * 8192 + 1024));
    };
    auto rdB = [&](u32x4* B, unsigned sb, int s) {
      #pragma unroll
      for (int t = 0; t < 3; ++t) B[t] = lds128(aB[s] + sb + t * 2048);
    };
    auto mm = [&](u32x4* A, u32x4* B) {
      #pragma unroll
      for (int i = 0; i < 8; ++i)
        #pragma unroll
        for (int t = 0; t < 3; ++t) acc[i][t] = mfma(A[i], B[t], acc[i][t]);
    };
    auto interleave = [&]() {
      #pragma unroll
      for (int i = 0; i < 19; ++i) { __builtin_amdgcn_sched_group_barrier(0x8, 1, 0); __builtin_amdgcn_sched_group_barrier(0x100, 1, 0); }
      __builtin_amdgcn_sched_group_barrier(0x8, 5, 0);
    };
    u64 tag1, tag2;
    asm volatile("s_load_dwordx2 %0, %2, 0x0 glc\n\ts_load_dwordx2 %1, %3, 0x0 glc\n\ts_waitcnt lgkmcnt(0)" : "=&s"(tag1), "=&s"(tag2) : "s"(pn1), "s"(pn2) : "memory");
    tag1 += 1; tag2 += 1;
    bar(); STAMP(1);
    rdB(B0, 0, 0); SB0; rdA(A0, 0, 0); SB0;
    unsigned sb = 0;
    for (int cc = 0; cc < 10; ++cc) {
      if (ABL == 1) { bar(); continue; }
      rdB(B1, sb, 1); rdA(A1, sb, 1);
      mm(A0, B0);
      interleave();
      SB0; waitlgkm0(); bar(); SB0;
      sb = sb == 0 ? 81920u : (sb == 81920u ? 40960u : 0u);
      if (cc < 9) { rdB(B0, sb, 0); rdA(A0, sb, 0); }
      mm(A1, B1);
      interleave();
    }
    STAMP(11);
    u32x4 qf[4][3];
    #pragma unroll
    for (int s = 0; s < 4; ++s)
      #pragma unroll
      for (int t = 0; t < 3; ++t) qf[s][t] = pack2(acc[2 * s][t], acc[2 * s + 1][t]);
    const unsigned Kb = L0 + ((w >> 1) ? L_K1 : L_K0) + c * 256 + 8 * (g & 1);
    f32x4 sc[6][3];
    #pragma unroll
    for (int kt = 0; kt < 6; ++kt) {
      #pragma unroll
      for (int t = 0; t < 3; ++t) sc[kt][t] = f32x4{0, 0, 0, 0};
      #pragma unroll
      for (int s = 0; s < 4; ++s) {
        const u32x4 af = cat(lds64(Kb + kt * 4096 + (((4 * s + (g >> 1)) ^ c) << 4)), lds64(Kb + kt * 4096 + (((4 * s + 2 + (g >> 1)) ^ c) << 4)));
        #pragma unroll
        for (int t = 0; t < 3; ++t) sc[kt][t] = mfma(af, qf[s][t], sc[kt][t]);
      }
    }
    const float SC = 0.08838834764831845f * 1.4426950408889634f;
    float inv[3];
    u32x4 pf[3][3];
    #pragma unroll
    for (int t = 0; t < 3; ++t) {
      float m = sc[0][t][0];
      #pragma unroll
      for (int kt = 0; kt < 6; ++kt)
        #pragma unroll
        for (int r = 0; r < 4; ++r) m = fmaxf(m, sc[kt][t][r]);
      m = xmax32(xmax16(m));
      const float ms = m * SC; float sum = 0;
      #pragma unroll
      for (int kt = 0; kt < 6; ++kt)
        #pragma unroll
        for (int r = 0; r < 4; ++r) { const float e = __builtin_amdgcn_exp2f(sc[kt][t][r] * SC - ms); sc[kt][t][r] = e; sum += e; }
      sum = xadd32(xadd16(sum));
      inv[t] = __builtin_amdgcn_rcpf(sum);
      #pragma unroll
      for (int s = 0; s < 3; ++s) pf[s][t] = pack2(sc[2 * s][t], sc[2 * s + 1][t]);
    }
    SB0; STAMP(13);
    waitlgkm0(); bar(); SB0;
    STAMP(14);
    const unsigned Vb = L0 + ((w >> 1) ? L_V1 : L_V0) + (4 * g + qq) * 256 + 8 * (p & 1);
    u32x4 of[4][3];
    {
      f32x4 o[8][3];
      #pragma unroll
      for (int i = 0; i < 8; ++i) {
        #pragma unroll
        for (int t = 0; t < 3; ++t) o[i][t] = f32x4{0, 0, 0, 0};
        const unsigned vb = Vb + (((2 * i + (p >> 1)) ^ (f8 << 1)) << 4);
        #pragma unroll
        for (int s = 0; s < 3; ++s) {
          const u32x4 af = cat(trread(vb + s * 8192), trread(vb + s * 8192 + 4096));
          #pragma unroll
          for (int t = 0; t < 3; ++t) o[i][t] = mfma(af, pf[s][t], o[i][t]);
        }
      }
      #pragma unroll
      for (int s = 0; s < 4; ++s)
        #pragma unroll
        for (int t = 0; t < 3; ++t) of[s][t] = pack2(o[2 * s][t] * inv[t], o[2 * s + 1][t] * inv[t]);
    }
    const rsrc_t rO = mkrsrc(ws + WS_O + (size_t)((br * 4 + bp) * 4 + w) * 32 * 12288, 32 * 12288);
    #pragma unroll
    for (int s = 0; s < 4; ++s)
      #pragma unroll
      for (int t = 0; t < 3; ++t) __builtin_amdgcn_raw_buffer_store_b128(of[s][t], rO, lane * 16 + (s * 3 + t) * 1024, h * 12288, 16);
    SB0; bar(); SB0;
    STAMP(15);
    waitvm<0>();
    if (lane == 0) __hip_atomic_store((GAS u64*)(ws + WS_F1 + (blockIdx.x * 4 + w) * FL), tag1, __ATOMIC_RELAXED, __HIP_MEMORY_SCOPE_AGENT);
    STAMP(16);
    bar();
    STAMP(17);
    f32x4 oc[5][3];
    #pragma unroll
    for (int m = 0; m < 5; ++m)
      #pragma unroll
      for (int t = 0; t < 3; ++t) oc[m][t] = f32x4{0, 0, 0, 0};
    constexpr int RING = RINGD;
    u32x4 bq[RING][3];
    const unsigned aW = L0 + (4 * g + qq) * 160 + 8 * p;
    const unsigned vl = lane * 16;
    auto bload = [&](int o) {
      const unsigned so = (hg * 8 + ((hl + (E2 == 1 ? 1 : (o >> 2))) & 7)) * 12288 + (o & 3) * 3072;
      if (E2 == 2) { bq[o % RING][0] = of[o & 3][0]; bq[o % RING][1] = of[o & 3][1]; bq[o % RING][2] = of[o & 3][2]; return; }
      asm volatile("buffer_load_dwordx4 %0, %1, %2, %3 offen sc1" : "=v"(bq[o % RING][0]) : "v"(vl), "s"(rO), "s"(so) : "memory");
      asm volatile("buffer_load_dwordx4 %0, %1, %2, %3 offen offset:1024 sc1" : "=v"(bq[o % RING][1]) : "v"(vl), "s"(rO), "s"(so) : "memory");
      asm volatile("buffer_load_dwordx4 %0, %1, %2, %3 offen offset:2048 sc1" : "=v"(bq[o % RING][2]) : "v"(vl), "s"(rO), "s"(so) : "memory");
    };
    u32x4 W[2][5];
    auto rdW = [&](int o) {
      #pragma unroll
      for (int m = 0; m < 5; ++m) {
        const unsigned ad = aW + (o >= 12 ? (o >= 24 ? 24 : 12) : 0) * 5120;
        const unsigned off = (o - (o >= 12 ? (o >= 24 ? 24 : 12) : 0)) * 5120 + 32 * m;
        W[o & 1][m] = cat(trread(ad + off), trread(ad + off + 16 * 160));
      }
    };
    auto kstep = [&](int o, const u32x4* B) {
      if (o < 31) rdW(o + 1);
      #pragma unroll
      for (int m = 0; m < 5; ++m)
        #pragma unroll
        for (int t = 0; t < 3; ++t) oc[m][t] = mfma(W[o & 1][m], B[t], oc[m][t]);
      #pragma unroll
      for (int i = 0; i < 10; ++i) { __builtin_amdgcn_sched_group_barrier(0x8, 1, 0); __builtin_amdgcn_sched_group_barrier(0x100, 1, 0); }
      __builtin_amdgcn_sched_group_barrier(0x8, 5, 0);
      SB0;
    };
    rdW(0);
    #pragma unroll
    for (int o = 4; o < 4 + RING - 1; ++o) bload(o);
    SB0;
    #pragma unroll
    for (int o = 0; o < 4; ++o) kstep(o, of[o]);
    auto pstep = [&](auto O) {
      constexpr int o = O.value;
      if (o + RING - 1 < 32) bload(o + RING - 1);
      constexpr int later = (o + RING - 1 < 32 ? RING - 1 : 31 - o) * 3;
      asm volatile("s_waitcnt vmcnt(%3)" : "+v"(bq[o % RING][0]), "+v"(bq[o % RING][1]), "+v"(bq[o % RING][2]) : "i"(later) : "memory");
      SB0;
      kstep(o, bq[o % RING]);
    };
    pstep(IC<4>{}); pstep(IC<5>{}); pstep(IC<6>{}); pstep(IC<7>{}); pstep(IC<8>{}); pstep(IC<9>{}); pstep(IC<10>{}); pstep(IC<11>{});
    pstep(IC<12>{}); pstep(IC<13>{}); pstep(IC<14>{}); pstep(IC<15>{}); pstep(IC<16>{}); pstep(IC<17>{}); pstep(IC<18>{}); pstep(IC<19>{});
    pstep(IC<20>{}); pstep(IC<21>{}); pstep(IC<22>{}); pstep(IC<23>{}); pstep(IC<24>{}); pstep(IC<25>{}); pstep(IC<26>{}); pstep(IC<27>{});
    pstep(IC<28>{}); pstep(IC<29>{}); pstep(IC<30>{}); pstep(IC<31>{});
    STAMP(18);
    #pragma unroll
    for (int t = 0; t < 3; ++t)
      #pragma unroll
      for (int m = 0; m < 4; m += 2)
        #pragma unroll
        for (int r = 0; r < 4; ++r) {
          auto x = __builtin_amdgcn_permlane16_swap(__float_as_uint(oc[m][t][r]), __float_as_uint(oc[m + 1][t][r]), false, false);
          oc[m][t][r] = __uint_as_float(x[0]); oc[m + 1][t][r] = __uint_as_float(x[1]);
        }
    f32x4 e4[3];
    #pragma unroll
    for (int t = 0; t < 3; ++t)
      #pragma unroll
      for (int r = 0; r < 4; ++r) {
        auto x = __builtin_amdgcn_permlane16_swap(__float_as_uint(oc[4][t][r]), __float_as_uint(oc[4][t][r]), false, false);
        oc[4][t][r] = __uint_as_float(x[0]); e4[t][r] = __uint_as_float(x[1]);
      }
    const rsrc_t rP = mkrsrc(ws + WS_P + (size_t)((((br * 4 + bp) * 8 + ns) * 4 + w) * 3) * 4 * 2560, 3 * 4 * 2560);
    const unsigned v4 = (g & 1) ? 0x80000000u : 2048 + ((g >> 1) * 16 + c) * 16;
    const unsigned tme = (hg + 3 - w) & 3;
    #pragma unroll
    for (int t = 0; t < 3; ++t) {
      if (tme != (unsigned)t) {
        const unsigned so = (t * 4 + hg) * 2560;
        __builtin_amdgcn_raw_buffer_store_b128(pack2(oc[0][t], oc[1][t]), rP, lane * 16, so, 16);
        __builtin_amdgcn_raw_buffer_store_b128(pack2(oc[2][t], oc[3][t]), rP, lane * 16 + 1024, so, 16);
        __builtin_amdgcn_raw_buffer_store_b128(pack2(oc[4][t], e4[t]), rP, v4, so, 16);
      }
    }
    waitvm<0>();
    if (lane == 0) __hip_atomic_store((GAS u64*)(ws + WS_F2 + (blockIdx.x * 4 + w) * FL), tag2, __ATOMIC_RELAXED, __HIP_MEMORY_SCOPE_AGENT);
    STAMP(19);
    if (tme < 3) {
      const unsigned peer = idx * 8 + br * 4 + ((hg + 1 + lane % 3) & 3);
      GAS u64* f = (GAS u64*)(ws + WS_F2 + (peer * 4 + w) * FL);
      for (unsigned s = 0; s < SPIN_CAP; ++s) {
        const u64 v = __hip_atomic_load(f, __ATOMIC_RELAXED, __HIP_MEMORY_SCOPE_AGENT);
        if (__all(v == tag2)) break;
        __builtin_amdgcn_s_sleep(1);
      }
      asm volatile("" ::: "memory");
      STAMP(20);
      u32x4 in[3][3];
      #pragma unroll
      for (int j = 0; j < 3; ++j) {
        const unsigned so = (tme * 4 + ((hg + 1 + j) & 3)) * 2560;
        in[j][0] = __builtin_amdgcn_raw_buffer_load_b128(rP, lane * 16, so, 16);
        in[j][1] = __builtin_amdgcn_raw_buffer_load_b128(rP, lane * 16 + 1024, so, 16);
        in[j][2] = __builtin_amdgcn_raw_buffer_load_b128(rP, v4, so, 16);
      }
      f32x4 y[6];
      #pragma unroll
      for (int m = 0; m < 5; ++m) y[m] = tme == 0 ? oc[m][0] : (tme == 1 ? oc[m][1] : oc[m][2]);
      y[5] = tme == 0 ? e4[0] : (tme == 1 ? e4[1] : e4[2]);
      #pragma unroll
      for (int j = 0; j < 3; ++j)
        #pragma unroll
        for (int u = 0; u < 3; ++u)
          #pragma unroll
          for (int d = 0; d < 4; ++d) {
            y[2 * u + (d >> 1)][2 * (d & 1)] += __uint_as_float(in[j][u][d] << 16);
            y[2 * u + (d >> 1)][2 * (d & 1) + 1] += __uint_as_float(in[j][u][d] & 0xffff0000u);
          }
      const rsrc_t rY = mkrsrc(a.out + (size_t)(bp * 192 + w * 48 + tme * 16) * 2560 + br * 1280 + ns * 160, 16 * 2560);
      const unsigned vy = c * 2560 + (g & 1) * 32 + (g >> 1) * 16;
      __builtin_amdgcn_raw_buffer_store_b128(pack2(y[0], y[1]), rY, vy, 0, 0);
      __builtin_amdgcn_raw_buffer_store_b128(pack2(y[2], y[3]), rY, vy + 64, 0, 0);
      __builtin_amdgcn_raw_buffer_store_b128(pack2(y[4], y[5]), rY, (g & 1) ? 0x80000000u : c * 2560 + 128 + (g >> 1) * 16, 0, 0);
      if (tme == 0 && lane == 0) __hip_atomic_store((GAS u64*)(ws + WS_N2 + (((br * 4 + bp) * 8 + ns) * 4 + w) * FL), tag2, __ATOMIC_RELAXED, __HIP_MEMORY_SCOPE_AGENT);
    }
    STAMP(21);
    if (blockIdx.x == 0 && wv == 0 && lane == 0) { __hip_atomic_store((GAS u64*)(ws + (40u << 20)), sp0, __ATOMIC_RELAXED, __HIP_MEMORY_SCOPE_AGENT); }
    while (__builtin_amdgcn_s_memrealtime() < sp0 + 4000) __builtin_amdgcn_s_sleep(2);
    sp0 = __hip_atomic_load((GAS u64*)(ws + (40u << 20)), __ATOMIC_RELAXED, __HIP_MEMORY_SCOPE_AGENT); sp1 = sp0;
    { u64 now = __builtin_amdgcn_s_memrealtime(); if (sp0 > now || now - sp0 > 100000) sp0 = sp1 = now; }
    { const u64 tgt = sp0 + 5000 + 4 * (sp1 - sp0); while (__builtin_amdgcn_s_memrealtime() < tgt) __builtin_amdgcn_s_sleep(2); }
  }
}

extern "C" void kernel_launch(void* const* d_in, const int* in_sizes, int n_in,
                              void* d_out, int out_size, void* d_ws, size_t ws_size,
                              hipStream_t stream) {
  Args a;
  for (int i = 0; i < 2; ++i) {
    a.x[i] = (const char*)d_in[5 * i + 0]; a.k[i] = (const char*)d_in[5 * i + 1]; a.v[i] = (const char*)d_in[5 * i + 2];
    a.wq[i] = (const char*)d_in[5 * i + 3]; a.wo[i] = (const char*)d_in[5 * i + 4];
  }
  a.ws = (char*)d_ws; a.out = (char*)d_out;
  (void)hipFuncSetAttribute((const void*)fused, hipFuncAttributeMaxDynamicSharedMemorySize, 163840);
  fused<<<256, 512, 163840, stream>>>(a);
}
```

```cpp
#include <hip/hip_runtime.h>
#include <hip/hip_bf16.h>

struct Args { const void* in[10]; void* ws; void* out; };

__global__ __launch_bounds__(512, 2) void fused(Args a) {
  asm volatile(
    "s_mov_b64 s[0:1], %0\n"
    "s_mov_b32 s2, %1\n"
    "v_mov_b32 v0, %2\n"
    ".Lfz_start:\n"
    "s_bfe_u32 s5, s2, 0x10002\n"
    "s_mul_i32 s50, s5, 40\n"
    "s_load_dwordx8 s[40:47], s[0:1], s50\n"
    "s_add_u32 s51, s50, 32\n"
    "s_load_dwordx2 s[48:49], s[0:1], s51\n"
    "s_load_dwordx4 s[32:35], s[0:1], 0x50\n"
    "v_readfirstlane_b32 s3, v0\n"
    "s_and_b32 s6, s2, 3\n"
    "s_bfe_u32 s7, s2, 0x30003\n"
    "s_lshr_b32 s8, s2, 6\n"
    "s_lshr_b32 s3, s3, 6\n"
    "s_and_b32 s4, s3, 3\n"
    "s_lshl_b32 s10, s6, 3\n"
    "s_or_b32 s10, s10, s7\n"
    "s_lshl_b32 s9, s8, 1\n"
    "s_sub_u32 s9, s7, s9\n"
    "s_and_b32 s9, s9, 7\n"
    "v_and_b32 v1, 63, v0\n"
    "v_and_b32 v2, 15, v0\n"
    "v_bfe_u32 v3, v0, 4, 2\n"
    "s_getpc_b64 s[86:87]\n"
    ".Lfz_pc:\n"
    "s_and_b32 s50, s2, 0xf8\n"
    "s_add_u32 s50, s50, s3\n"
    "s_lshl_b32 s50, s50, 7\n"
    "s_min_u32 s50, s50, .Lfz_end-.Lfz_start\n"
    "v_mov_b32 v109, s50\n"
    "s_sub_u32 s86, s86, .Lfz_pc-.Lfz_start\n"
    "s_subb_u32 s87, s87, 0\n"
    "s_mov_b64 exec, 1\n"
    "global_load_dword v108, v109, s[86:87]\n"
    "s_mov_b64 exec, -1\n"
    "s_cmp_lt_u32 s3, 4\n"
    "s_cbranch_scc1 .Lfz_compute\n"
    "v_lshlrev_b32 v28, 1, v3\n"
    "v_xor_b32 v28, v28, v2\n"
    "v_lshlrev_b32 v28, 4, v28\n"
    "v_lshlrev_b32 v29, 13, v3\n"
    "v_add_u32 v10, v29, v28\n"
    "v_xor_b32 v30, 0x80, v28\n"
    "v_add_u32 v12, v29, v30\n"
    "v_add_u32 v11, 35840, v10\n"
    "v_add_u32 v13, 99328, v12\n"
    "v_add_u32 v12, 67584, v12\n"
    "v_add_u32 v10, 4096, v10\n"
    "v_lshlrev_b32 v29, 8, v3\n"
    "v_add_u32 v14, v29, v28\n"
    "v_add_u32 v15, v29, v30\n"
    "v_lshrrev_b32 v29, 3, v1\n"
    "v_mul_u32_u24 v29, 1280, v29\n"
    "v_and_b32 v28, 7, v1\n"
    "v_xor_b32 v28, v28, v3\n"
    "v_lshlrev_b32 v28, 4, v28\n"
    "v_xor_b32 v30, 64, v28\n"
    "v_add_u32 v28, v29, v28\n"
    "v_add_u32 v30, v29, v30\n"
    "v_add_u32 v4, 4096, v28\n"
    "v_add_u32 v5, 13312, v30\n"
    "v_add_u32 v6, 22528, v28\n"
    "v_add_u32 v7, 31744, v30\n"
    "v_add_u32 v8, 45056, v28\n"
    "v_add_u32 v9, 54272, v30\n"
    "s_mul_i32 s52, s8, 192\n"
    "s_mul_i32 s50, s4, 48\n"
    "s_add_u32 s52, s52, s50\n"
    "s_mul_i32 s52, s52, 1280\n"
    "s_lshl_b32 s53, s4, 17\n"
    "s_lshl_b32 s50, s10, 8\n"
    "s_add_u32 s53, s53, s50\n"
    "s_lshl_b32 s54, s4, 12\n"
    "s_mul_i32 s55, s4, 6144\n"
    "s_add_u32 s59, s8, s7\n"
    "s_and_b32 s59, s59, 3\n"
    "s_mul_i32 s59, s59, 8\n"
    "s_lshr_b32 s59, s59, 2\n"
    "s_waitcnt lgkmcnt(0)\n"
    "s_sub_u32 s16, s46, 4096\n"
    "s_subb_u32 s17, s47, 0\n"
    "s_and_b32 s17, s17, 0xffff\n"
    "s_mov_b32 s18, 5246976\n"
    "s_mov_b32 s19, 0x20000\n"
    "s_sub_u32 s12, s40, 4096\n"
    "s_subb_u32 s13, s41, 0\n"
    "s_and_b32 s13, s13, 0xffff\n"
    "s_mov_b32 s14, 987136\n"
    "s_mov_b32 s15, 0x20000\n"
    "s_mov_b64 s[20:21], s[42:43]\n"
    "s_mov_b32 s56, 0\n"
    "s_lshl_b32 s50, s56, 19\n"
    "s_add_u32 s50, s50, s53\n"
    "s_lshl_b32 s51, s56, 7\n"
    "s_add_u32 s51, s51, s52\n"
    "s_add_u32 m0, s54, 40960\n"
    "s_nop 0\n"
    "buffer_load_dwordx4 v10, s[16:19], s50 offen offset:0 nt lds\n"
    "buffer_load_dwordx4 v11, s[16:19], s50 offen offset:1024 nt lds\n"
    "buffer_load_dwordx4 v12, s[16:19], s50 offen offset:2048 nt lds\n"
    "buffer_load_dwordx4 v13, s[16:19], s50 offen offset:3072 nt lds\n"
    "s_add_u32 m0, s55, 57344\n"
    "s_nop 0\n"
    "buffer_load_dwordx4 v4, s[12:15], s51 offen offset:0 lds\n"
    "buffer_load_dwordx4 v5, s[12:15], s51 offen offset:1024 lds\n"
    "buffer_load_dwordx4 v6, s[12:15], s51 offen offset:2048 lds\n"
    "buffer_load_dwordx4 v7, s[12:15], s51 offen offset:3072 lds\n"
    "s_add_u32 m0, s55, 61440\n"
    "s_nop 0\n"
    "buffer_load_dwordx4 v8, s[12:15], s51 offen offset:0 lds\n"
    "buffer_load_dwordx4 v9, s[12:15], s51 offen offset:1024 lds\n"
    "s_lshl_b32 s50, s5, 2\n"
    "s_or_b32 s50, s50, s6\n"
    "s_lshl_b32 s50, s50, 2\n"
    "s_or_b32 s50, s50, s8\n"
    "s_lshl_b32 s50, s50, 2\n"
    "s_or_b32 s50, s50, s4\n"
    "s_lshl_b32 s50, s50, 7\n"
    "s_add_u32 s90, s50, 20709376\n"
    "s_load_dwordx2 s[36:37], s[32:33], s90 glc\n"
    "s_lshl_b32 s50, s5, 2\n"
    "s_or_b32 s50, s50, s8\n"
    "s_lshl_b32 s50, s50, 3\n"
    "s_or_b32 s50, s50, s9\n"
    "s_lshl_b32 s50, s50, 2\n"
    "s_or_b32 s98, s50, s4\n"
    "s_lshl_b32 s50, s98, 7\n"
    "s_add_u32 s99, s50, 20725760\n"
    "s_load_dwordx2 s[38:39], s[32:33], s99 glc\n"
    "s_and_b32 s21, s21, 0xffff\n"
    "s_mov_b32 s22, 6291456\n"
    "s_mov_b32 s23, 0x20000\n"
    "s_mov_b32 s24, s44\n"
    "s_and_b32 s25, s45, 0xffff\n"
    "s_mov_b32 s26, 6291456\n"
    "s_mov_b32 s27, 0x20000\n"
    "s_sub_u32 s28, s48, 4096\n"
    "s_subb_u32 s29, s49, 0\n"
    "s_and_b32 s29, s29, 0xffff\n"
    "s_mov_b32 s30, 5246976\n"
    "s_mov_b32 s31, 0x20000\n"
    "s_lshl_b32 s57, s8, 6\n"
    "s_add_u32 s57, s57, s10\n"
    "s_mul_i32 s57, s57, 24576\n"
    "s_add_u32 s57, s57, s55\n"
    "s_add_u32 s58, s57, 786432\n"
    "v_lshlrev_b32 v27, 4, v1\n"
    "s_mul_i32 s50, s4, 24\n"
    "v_add_u32 v28, s50, v3\n"
    "v_add_u32 v29, 0, v28\n"
    "v_xor_b32 v30, v29, v2\n"
    "v_and_b32 v30, 15, v30\n"
    "v_lshlrev_b32 v30, 4, v30\n"
    "v_lshl_add_u32 v30, v29, 8, v30\n"
    "v_add_u32 v21, 65280, v30\n"
    "v_add_u32 v29, 4, v28\n"
    "v_xor_b32 v30, v29, v2\n"
    "v_and_b32 v30, 15, v30\n"
    "v_lshlrev_b32 v30, 4, v30\n"
    "v_lshl_add_u32 v30, v29, 8, v30\n"
    "v_add_u32 v22, 65280, v30\n"
    "v_add_u32 v29, 8, v28\n"
    "v_xor_b32 v30, v29, v2\n"
    "v_and_b32 v30, 15, v30\n"
    "v_lshlrev_b32 v30, 4, v30\n"
    "v_lshl_add_u32 v30, v29, 8, v30\n"
    "v_add_u32 v23, 65280, v30\n"
    "v_add_u32 v29, 12, v28\n"
    "v_xor_b32 v30, v29, v2\n"
    "v_and_b32 v30, 15, v30\n"
    "v_lshlrev_b32 v30, 4, v30\n"
    "v_lshl_add_u32 v30, v29, 8, v30\n"
    "v_add_u32 v24, 65280, v30\n"
    "v_add_u32 v29, 16, v28\n"
    "v_xor_b32 v30, v29, v2\n"
    "v_and_b32 v30, 15, v30\n"
    "v_lshlrev_b32 v30, 4, v30\n"
    "v_lshl_add_u32 v30, v29, 8, v30\n"
    "v_add_u32 v25, 65280, v30\n"
    "v_add_u32 v29, 20, v28\n"
    "v_xor_b32 v30, v29, v2\n"
    "v_and_b32 v30, 15, v30\n"
    "v_lshlrev_b32 v30, 4, v30\n"
    "v_lshl_add_u32 v30, v29, 8, v30\n"
    "v_add_u32 v26, 65280, v30\n"
    "v_add_u32 v28, 0, v1\n"
    "v_mul_u32_u24 v29, 6554, v28\n"
    "v_lshrrev_b32 v29, 16, v29\n"
    "v_mul_u32_u24 v30, 10, v29\n"
    "v_sub_u32 v28, v28, v30\n"
    "v_mul_u32_u24 v29, 1280, v29\n"
    "v_lshl_add_u32 v28, v28, 4, v29\n"
    "v_add_u32 v16, 4096, v28\n"
    "v_add_u32 v28, 64, v1\n"
    "v_mul_u32_u24 v29, 6554, v28\n"
    "v_lshrrev_b32 v29, 16, v29\n"
    "v_mul_u32_u24 v30, 10, v29\n"
    "v_sub_u32 v28, v28, v30\n"
    "v_mul_u32_u24 v29, 1280, v29\n"
    "v_lshl_add_u32 v28, v28, 4, v29\n"
    "v_add_u32 v17, 3072, v28\n"
    "v_add_u32 v28, 128, v1\n"
    "v_mul_u32_u24 v29, 6554, v28\n"
    "v_lshrrev_b32 v29, 16, v29\n"
    "v_mul_u32_u24 v30, 10, v29\n"
    "v_sub_u32 v28, v28, v30\n"
    "v_mul_u32_u24 v29, 1280, v29\n"
    "v_lshl_add_u32 v28, v28, 4, v29\n"
    "v_add_u32 v18, 2048, v28\n"
    "v_add_u32 v28, 192, v1\n"
    "v_mul_u32_u24 v29, 6554, v28\n"
    "v_lshrrev_b32 v29, 16, v29\n"
    "v_mul_u32_u24 v30, 10, v29\n"
    "v_sub_u32 v28, v28, v30\n"
    "v_mul_u32_u24 v29, 1280, v29\n"
    "v_lshl_add_u32 v28, v28, 4, v29\n"
    "v_add_u32 v19, 1024, v28\n"
    "v_add_u32 v28, 256, v1\n"
    "v_mul_u32_u24 v29, 6554, v28\n"
    "v_lshrrev_b32 v29, 16, v29\n"
    "v_mul_u32_u24 v30, 10, v29\n"
    "v_sub_u32 v28, v28, v30\n"
    "v_mul_u32_u24 v29, 1280, v29\n"
    "v_lshl_add_u32 v28, v28, 4, v29\n"
    "v_add_u32 v20, 4096, v28\n"
    "s_lshl_b32 s88, s6, 10\n"
    "s_lshl_b32 s50, s4, 5\n"
    "s_add_u32 s88, s88, s50\n"
    "s_mul_i32 s88, s88, 1280\n"
    "s_mul_i32 s50, s9, 160\n"
    "s_add_u32 s88, s88, s50\n"
    "s_mul_i32 s89, s4, 5120\n"
    "v_min_u32 v28, 6, v1\n"
    "s_add_u32 s50, s7, 1\n"
    "v_add_u32 v28, s50, v28\n"
    "v_and_b32 v28, 7, v28\n"
    "s_lshl_b32 s50, s8, 6\n"
    "s_and_b32 s51, s2, 7\n"
    "s_add_u32 s50, s50, s51\n"
    "s_lshl_b32 s50, s50, 2\n"
    "s_add_u32 s50, s50, s4\n"
    "s_lshl_b32 s50, s50, 7\n"
    "s_add_u32 s50, s50, 20447232\n"
    "v_lshlrev_b32 v28, 12, v28\n"
    "v_add_u32 v82, s50, v28\n"
    "s_mov_b32 s56, 1\n"
    "s_lshl_b32 s50, s56, 19\n"
    "s_add_u32 s50, s50, s53\n"
    "s_lshl_b32 s51, s56, 7\n"
    "s_add_u32 s51, s51, s52\n"
    "s_add_u32 m0, s54, 0\n"
    "s_nop 0\n"
    "buffer_load_dwordx4 v10, s[16:19], s50 offen offset:0 nt lds\n"
    "buffer_load_dwordx4 v11, s[16:19], s50 offen offset:1024 nt lds\n"
    "buffer_load_dwordx4 v12, s[16:19], s50 offen offset:2048 nt lds\n"
    "buffer_load_dwordx4 v13, s[16:19], s50 offen offset:3072 nt lds\n"
    "s_add_u32 m0, s55, 16384\n"
    "s_nop 0\n"
    "buffer_load_dwordx4 v4, s[12:15], s51 offen offset:0 lds\n"
    "buffer_load_dwordx4 v5, s[12:15], s51 offen offset:1024 lds\n"
    "buffer_load_dwordx4 v6, s[12:15], s51 offen offset:2048 lds\n"
    "buffer_load_dwordx4 v7, s[12:15], s51 offen offset:3072 lds\n"
    "s_add_u32 m0, s55, 20480\n"
    "s_nop 0\n"
    "buffer_load_dwordx4 v8, s[12:15], s51 offen offset:0 lds\n"
    "buffer_load_dwordx4 v9, s[12:15], s51 offen offset:1024 lds\n"
    "s_waitcnt vmcnt(10)\n"
    "s_barrier\n"
    "s_lshr_b32 s50, s7, 3\n"
    "s_add_u32 s50, s50, s8\n"
    "s_and_b32 s50, s50, 1\n"
    "s_cmp_eq_u32 s50, 1\n"
    "s_cbranch_scc1 .Lfz_var1\n"
    "s_add_u32 s56, s59, 2\n"
    "s_lshl_b32 s50, s56, 19\n"
    "s_add_u32 s50, s50, s53\n"
    "s_lshl_b32 s51, s56, 7\n"
    "s_add_u32 s51, s51, s52\n"
    "s_add_u32 m0, s54, 122880\n"
    "s_nop 0\n"
    "buffer_load_dwordx4 v10, s[16:19], s50 offen offset:0 lds\n"
    "buffer_load_dwordx4 v11, s[16:19], s50 offen offset:1024 lds\n"
    "buffer_load_dwordx4 v12, s[16:19], s50 offen offset:2048 lds\n"
    "buffer_load_dwordx4 v13, s[16:19], s50 offen offset:3072 lds\n"
    "s_add_u32 m0, s55, 139264\n"
    "s_nop 0\n"
    "buffer_load_dwordx4 v4, s[12:15], s51 offen offset:0 lds\n"
    "buffer_load_dwordx4 v5, s[12:15], s51 offen offset:1024 lds\n"
    "buffer_load_dwordx4 v6, s[12:15], s51 offen offset:2048 lds\n"
    "buffer_load_dwordx4 v7, s[12:15], s51 offen offset:3072 lds\n"
    "s_add_u32 m0, s55, 143360\n"
    "s_nop 0\n"
    "buffer_load_dwordx4 v8, s[12:15], s51 offen offset:0 lds\n"
    "buffer_load_dwordx4 v9, s[12:15], s51 offen offset:1024 lds\n"
    "s_waitcnt vmcnt(10)\n"
    "s_barrier\n"
    "s_add_u32 s56, s56, 1\n"
    "s_cmp_eq_u32 s56, 10\n"
    "s_cselect_b32 s56, 2, s56\n"
    "s_lshl_b32 s50, s56, 19\n"
    "s_add_u32 s50, s50, s53\n"
    "s_lshl_b32 s51, s56, 7\n"
    "s_add_u32 s51, s51, s52\n"
    "s_add_u32 m0, s54, 81920\n"
    "s_nop 0\n"
    "buffer_load_dwordx4 v10, s[16:19], s50 offen offset:0 lds\n"
    "buffer_load_dwordx4 v11, s[16:19], s50 offen offset:1024 lds\n"
    "buffer_load_dwordx4 v12, s[16:19], s50 offen offset:2048 lds\n"
    "buffer_load_dwordx4 v13, s[16:19], s50 offen offset:3072 lds\n"
    "s_add_u32 m0, s55, 98304\n"
    "s_nop 0\n"
    "buffer_load_dwordx4 v4, s[12:15], s51 offen offset:0 lds\n"
    "buffer_load_dwordx4 v5, s[12:15], s51 offen offset:1024 lds\n"
    "buffer_load_dwordx4 v6, s[12:15], s51 offen offset:2048 lds\n"
    "buffer_load_dwordx4 v7, s[12:15], s51 offen offset:3072 lds\n"
    "s_add_u32 m0, s55, 102400\n"
    "s_nop 0\n"
    "buffer_load_dwordx4 v8, s[12:15], s51 offen offset:0 lds\n"
    "buffer_load_dwordx4 v9, s[12:15], s51 offen offset:1024 lds\n"
    "s_add_u32 s56, s56, 1\n"
    "s_cmp_eq_u32 s56, 10\n"
    "s_cselect_b32 s56, 2, s56\n"
    "s_lshl_b32 s50, s56, 19\n"
    "s_add_u32 s50, s50, s53\n"
    "s_lshl_b32 s51, s56, 7\n"
    "s_add_u32 s51, s51, s52\n"
    "s_add_u32 m0, s54, 40960\n"
    "s_nop 0\n"
    "buffer_load_dwordx4 v10, s[16:19], s50 offen offset:0 lds\n"
    "buffer_load_dwordx4 v11, s[16:19], s50 offen offset:1024 lds\n"
    "buffer_load_dwordx4 v12, s[16:19], s50 offen offset:2048 lds\n"
    "buffer_load_dwordx4 v13, s[16:19], s50 offen offset:3072 lds\n"
    "s_add_u32 m0, s55, 57344\n"
    "s_nop 0\n"
    "buffer_load_dwordx4 v4, s[12:15], s51 offen offset:0 lds\n"
    "buffer_load_dwordx4 v5, s[12:15], s51 offen offset:1024 lds\n"
    "buffer_load_dwordx4 v6, s[12:15], s51 offen offset:2048 lds\n"
    "buffer_load_dwordx4 v7, s[12:15], s51 offen offset:3072 lds\n"
    "s_add_u32 m0, s55, 61440\n"
    "s_nop 0\n"
    "buffer_load_dwordx4 v8, s[12:15], s51 offen offset:0 lds\n"
    "buffer_load_dwordx4 v9, s[12:15], s51 offen offset:1024 lds\n"
    "s_waitcnt vmcnt(20)\n"
    "s_barrier\n"
    "s_add_u32 s56, s56, 1\n"
    "s_cmp_eq_u32 s56, 10\n"
    "s_cselect_b32 s56, 2, s56\n"
    "s_lshl_b32 s50, s56, 19\n"
    "s_add_u32 s50, s50, s53\n"
    "s_lshl_b32 s51, s56, 7\n"
    "s_add_u32 s51, s51, s52\n"
    "s_add_u32 m0, s54, 0\n"
    "s_nop 0\n"
    "buffer_load_dwordx4 v10, s[16:19], s50 offen offset:0 lds\n"
    "buffer_load_dwordx4 v11, s[16:19], s50 offen offset:1024 lds\n"
    "buffer_load_dwordx4 v12, s[16:19], s50 offen offset:2048 lds\n"
    "buffer_load_dwordx4 v13, s[16:19], s50 offen offset:3072 lds\n"
    "s_add_u32 m0, s55, 16384\n"
    "s_nop 0\n"
    "buffer_load_dwordx4 v4, s[12:15], s51 offen offset:0 lds\n"
    "buffer_load_dwordx4 v5, s[12:15], s51 offen offset:1024 lds\n"
    "buffer_load_dwordx4 v6, s[12:15], s51 offen offset:2048 lds\n"
    "buffer_load_dwordx4 v7, s[12:15], s51 offen offset:3072 lds\n"
    "s_add_u32 m0, s55, 20480\n"
    "s_nop 0\n"
    "buffer_load_dwordx4 v8, s[12:15], s51 offen offset:0 lds\n"
    "buffer_load_dwordx4 v9, s[12:15], s51 offen offset:1024 lds\n"
    "s_add_u32 s50, s57, 4096\n"
    "buffer_load_dwordx4 v[32:35], v27, s[20:23], s57 offen offset:0 nt\n"
    "buffer_load_dwordx4 v[36:39], v27, s[20:23], s57 offen offset:1024 nt\n"
    "buffer_load_dwordx4 v[40:43], v27, s[20:23], s57 offen offset:2048 nt\n"
    "buffer_load_dwordx4 v[44:47], v27, s[20:23], s57 offen offset:3072 nt\n"
    "buffer_load_dwordx4 v[48:51], v27, s[20:23], s50 offen offset:0 nt\n"
    "buffer_load_dwordx4 v[52:55], v27, s[20:23], s50 offen offset:1024 nt\n"
    "s_waitcnt vmcnt(26)\n"
    "s_barrier\n"
    "s_add_u32 s56, s56, 1\n"
    "s_cmp_eq_u32 s56, 10\n"
    "s_cselect_b32 s56, 2, s56\n"
    "s_lshl_b32 s50, s56, 19\n"
    "s_add_u32 s50, s50, s53\n"
    "s_lshl_b32 s51, s56, 7\n"
    "s_add_u32 s51, s51, s52\n"
    "s_add_u32 m0, s54, 122880\n"
    "s_nop 0\n"
    "buffer_load_dwordx4 v10, s[16:19], s50 offen offset:0 lds\n"
    "buffer_load_dwordx4 v11, s[16:19], s50 offen offset:1024 lds\n"
    "buffer_load_dwordx4 v12, s[16:19], s50 offen offset:2048 lds\n"
    "buffer_load_dwordx4 v13, s[16:19], s50 offen offset:3072 lds\n"
    "s_add_u32 m0, s55, 139264\n"
    "s_nop 0\n"
    "buffer_load_dwordx4 v4, s[12:15], s51 offen offset:0 lds\n"
    "buffer_load_dwordx4 v5, s[12:15], s51 offen offset:1024 lds\n"
    "buffer_load_dwordx4 v6, s[12:15], s51 offen offset:2048 lds\n"
    "buffer_load_dwordx4 v7, s[12:15], s51 offen offset:3072 lds\n"
    "s_add_u32 m0, s55, 143360\n"
    "s_nop 0\n"
    "buffer_load_dwordx4 v8, s[12:15], s51 offen offset:0 lds\n"
    "buffer_load_dwordx4 v9, s[12:15], s51 offen offset:1024 lds\n"
    "s_waitcnt vmcnt(26)\n"
    "s_barrier\n"
    "s_add_u32 s56, s56, 1\n"
    "s_cmp_eq_u32 s56, 10\n"
    "s_cselect_b32 s56, 2, s56\n"
    "s_lshl_b32 s50, s56, 19\n"
    "s_add_u32 s50, s50, s53\n"
    "s_lshl_b32 s51, s56, 7\n"
    "s_add_u32 s51, s51, s52\n"
    "s_add_u32 m0, s54, 81920\n"
    "s_nop 0\n"
    "buffer_load_dwordx4 v10, s[16:19], s50 offen offset:0 lds\n"
    "buffer_load_dwordx4 v11, s[16:19], s50 offen offset:1024 lds\n"
    "buffer_load_dwordx4 v12, s[16:19], s50 offen offset:2048 lds\n"
    "buffer_load_dwordx4 v13, s[16:19], s50 offen offset:3072 lds\n"
    "s_add_u32 m0, s55, 98304\n"
    "s_nop 0\n"
    "buffer_load_dwordx4 v4, s[12:15], s51 offen offset:0 lds\n"
    "buffer_load_dwordx4 v5, s[12:15], s51 offen offset:1024 lds\n"
    "buffer_load_dwordx4 v6, s[12:15], s51 offen offset:2048 lds\n"
    "buffer_load_dwordx4 v7, s[12:15], s51 offen offset:3072 lds\n"
    "s_add_u32 m0, s55, 102400\n"
    "s_nop 0\n"
    "buffer_load_dwordx4 v8, s[12:15], s51 offen offset:0 lds\n"
    "buffer_load_dwordx4 v9, s[12:15], s51 offen offset:1024 lds\n"
    "s_add_u32 s50, s58, 4096\n"
    "buffer_load_dwordx4 v[56:59], v27, s[20:23], s58 offen offset:0 nt\n"
    "buffer_load_dwordx4 v[60:63], v27, s[20:23], s58 offen offset:1024 nt\n"
    "buffer_load_dwordx4 v[64:67], v27, s[20:23], s58 offen offset:2048 nt\n"
    "buffer_load_dwordx4 v[68:71], v27, s[20:23], s58 offen offset:3072 nt\n"
    "buffer_load_dwordx4 v[72:75], v27, s[20:23], s50 offen offset:0 nt\n"
    "buffer_load_dwordx4 v[76:79], v27, s[20:23], s50 offen offset:1024 nt\n"
    "s_waitcnt vmcnt(32)\n"
    "s_barrier\n"
    "s_add_u32 s56, s56, 1\n"
    "s_cmp_eq_u32 s56, 10\n"
    "s_cselect_b32 s56, 2, s56\n"
    "s_lshl_b32 s50, s56, 19\n"
    "s_add_u32 s50, s50, s53\n"
    "s_lshl_b32 s51, s56, 7\n"
    "s_add_u32 s51, s51, s52\n"
    "s_add_u32 m0, s54, 40960\n"
    "s_nop 0\n"
    "buffer_load_dwordx4 v10, s[16:19], s50 offen offset:0 lds\n"
    "buffer_load_dwordx4 v11, s[16:19], s50 offen offset:1024 lds\n"
    "buffer_load_dwordx4 v12, s[16:19], s50 offen offset:2048 lds\n"
    "buffer_load_dwordx4 v13, s[16:19], s50 offen offset:3072 lds\n"
    "s_add_u32 m0, s55, 57344\n"
    "s_nop 0\n"
    "buffer_load_dwordx4 v4, s[12:15], s51 offen offset:0 lds\n"
    "buffer_load_dwordx4 v5, s[12:15], s51 offen offset:1024 lds\n"
    "buffer_load_dwordx4 v6, s[12:15], s51 offen offset:2048 lds\n"
    "buffer_load_dwordx4 v7, s[12:15], s51 offen offset:3072 lds\n"
    "s_add_u32 m0, s55, 61440\n"
    "s_nop 0\n"
    "buffer_load_dwordx4 v8, s[12:15], s51 offen offset:0 lds\n"
    "buffer_load_dwordx4 v9, s[12:15], s51 offen offset:1024 lds\n"
    "s_waitcnt vmcnt(26)\n"
    "s_barrier\n"
    "s_add_u32 s56, s56, 1\n"
    "s_cmp_eq_u32 s56, 10\n"
    "s_cselect_b32 s56, 2, s56\n"
    "s_lshl_b32 s50, s56, 19\n"
    "s_add_u32 s50, s50, s53\n"
    "s_lshl_b32 s51, s56, 7\n"
    "s_add_u32 s51, s51, s52\n"
    "s_add_u32 m0, s54, 0\n"
    "s_nop 0\n"
    "buffer_load_dwordx4 v10, s[16:19], s50 offen offset:0 lds\n"
    "buffer_load_dwordx4 v11, s[16:19], s50 offen offset:1024 lds\n"
    "buffer_load_dwordx4 v12, s[16:19], s50 offen offset:2048 lds\n"
    "buffer_load_dwordx4 v13, s[16:19], s50 offen offset:3072 lds\n"
    "s_add_u32 m0, s55, 16384\n"
    "s_nop 0\n"
    "buffer_load_dwordx4 v4, s[12:15], s51 offen offset:0 lds\n"
    "buffer_load_dwordx4 v5, s[12:15], s51 offen offset:1024 lds\n"
    "buffer_load_dwordx4 v6, s[12:15], s51 offen offset:2048 lds\n"
    "buffer_load_dwordx4 v7, s[12:15], s51 offen offset:3072 lds\n"
    "s_add_u32 m0, s55, 20480\n"
    "s_nop 0\n"
    "buffer_load_dwordx4 v8, s[12:15], s51 offen offset:0 lds\n"
    "buffer_load_dwordx4 v9, s[12:15], s51 offen offset:1024 lds\n"
    "s_waitcnt vmcnt(26)\n"
    "s_barrier\n"
    "s_add_u32 m0, s55, 139008\n"
    "s_add_u32 s50, s58, 4096\n"
    "buffer_load_dwordx4 v14, s[24:27], s58 offen offset:0 nt lds\n"
    "buffer_load_dwordx4 v15, s[24:27], s58 offen offset:1024 nt lds\n"
    "buffer_load_dwordx4 v14, s[24:27], s58 offen offset:2048 nt lds\n"
    "buffer_load_dwordx4 v15, s[24:27], s58 offen offset:3072 nt lds\n"
    "s_add_u32 m0, s55, 143104\n"
    "s_nop 0\n"
    "buffer_load_dwordx4 v14, s[24:27], s50 offen offset:0 nt lds\n"
    "buffer_load_dwordx4 v15, s[24:27], s50 offen offset:1024 nt lds\n"
    "s_waitcnt vmcnt(16)\n"
    "s_barrier\n"
    "s_waitcnt vmcnt(52)\n"
    "ds_write_b128 v21, v[32:35] offset:24576\n"
    "ds_write_b128 v22, v[36:39] offset:24576\n"
    "ds_write_b128 v23, v[40:43] offset:24576\n"
    "ds_write_b128 v24, v[44:47] offset:24576\n"
    "ds_write_b128 v25, v[48:51] offset:24576\n"
    "ds_write_b128 v26, v[52:55] offset:24576\n"
    "s_add_u32 m0, s55, 114432\n"
    "s_add_u32 s50, s57, 4096\n"
    "buffer_load_dwordx4 v14, s[24:27], s57 offen offset:0 nt lds\n"
    "buffer_load_dwordx4 v15, s[24:27], s57 offen offset:1024 nt lds\n"
    "buffer_load_dwordx4 v14, s[24:27], s57 offen offset:2048 nt lds\n"
    "buffer_load_dwordx4 v15, s[24:27], s57 offen offset:3072 nt lds\n"
    "s_add_u32 m0, s55, 118528\n"
    "s_nop 0\n"
    "buffer_load_dwordx4 v14, s[24:27], s50 offen offset:0 nt lds\n"
    "buffer_load_dwordx4 v15, s[24:27], s50 offen offset:1024 nt lds\n"
    "s_waitcnt vmcnt(12)\n"
    "s_barrier\n"
    "s_waitcnt vmcnt(32)\n"
    "ds_write_b128 v21, v[56:59] offset:0\n"
    "ds_write_b128 v22, v[60:63] offset:0\n"
    "ds_write_b128 v23, v[64:67] offset:0\n"
    "ds_write_b128 v24, v[68:71] offset:0\n"
    "ds_write_b128 v25, v[72:75] offset:0\n"
    "ds_write_b128 v26, v[76:79] offset:0\n"
    "s_waitcnt lgkmcnt(0)\n"
    "s_barrier\n"
    "s_branch .Lfz_varend\n"
    ".Lfz_var1:\n"
    "s_add_u32 s56, s59, 2\n"
    "s_lshl_b32 s50, s56, 19\n"
    "s_add_u32 s50, s50, s53\n"
    "s_lshl_b32 s51, s56, 7\n"
    "s_add_u32 s51, s51, s52\n"
    "s_add_u32 m0, s54, 122880\n"
    "s_nop 0\n"
    "buffer_load_dwordx4 v10, s[16:19], s50 offen offset:0 lds\n"
    "buffer_load_dwordx4 v11, s[16:19], s50 offen offset:1024 lds\n"
    "buffer_load_dwordx4 v12, s[16:19], s50 offen offset:2048 lds\n"
    "buffer_load_dwordx4 v13, s[16:19], s50 offen offset:3072 lds\n"
    "s_add_u32 m0, s55, 139264\n"
    "s_nop 0\n"
    "buffer_load_dwordx4 v4, s[12:15], s51 offen offset:0 lds\n"
    "buffer_load_dwordx4 v5, s[12:15], s51 offen offset:1024 lds\n"
    "buffer_load_dwordx4 v6, s[12:15], s51 offen offset:2048 lds\n"
    "buffer_load_dwordx4 v7, s[12:15], s51 offen offset:3072 lds\n"
    "s_add_u32 m0, s55, 143360\n"
    "s_nop 0\n"
    "buffer_load_dwordx4 v8, s[12:15], s51 offen offset:0 lds\n"
    "buffer_load_dwordx4 v9, s[12:15], s51 offen offset:1024 lds\n"
    "s_waitcnt vmcnt(10)\n"
    "s_barrier\n"
    "s_add_u32 s56, s56, 1\n"
    "s_cmp_eq_u32 s56, 10\n"
    "s_cselect_b32 s56, 2, s56\n"
    "s_lshl_b32 s50, s56, 19\n"
    "s_add_u32 s50, s50, s53\n"
    "s_lshl_b32 s51, s56, 7\n"
    "s_add_u32 s51, s51, s52\n"
    "s_add_u32 m0, s54, 81920\n"
    "s_nop 0\n"
    "buffer_load_dwordx4 v10, s[16:19], s50 offen offset:0 lds\n"
    "buffer_load_dwordx4 v11, s[16:19], s50 offen offset:1024 lds\n"
    "buffer_load_dwordx4 v12, s[16:19], s50 offen offset:2048 lds\n"
    "buffer_load_dwordx4 v13, s[16:19], s50 offen offset:3072 lds\n"
    "s_add_u32 m0, s55, 98304\n"
    "s_nop 0\n"
    "buffer_load_dwordx4 v4, s[12:15], s51 offen offset:0 lds\n"
    "buffer_load_dwordx4 v5, s[12:15], s51 offen offset:1024 lds\n"
    "buffer_load_dwordx4 v6, s[12:15], s51 offen offset:2048 lds\n"
    "buffer_load_dwordx4 v7, s[12:15], s51 offen offset:3072 lds\n"
    "s_add_u32 m0, s55, 102400\n"
    "s_nop 0\n"
    "buffer_load_dwordx4 v8, s[12:15], s51 offen offset:0 lds\n"
    "buffer_load_dwordx4 v9, s[12:15], s51 offen offset:1024 lds\n"
    "s_waitcnt vmcnt(10)\n"
    "s_barrier\n"
    "s_add_u32 s56, s56, 1\n"
    "s_cmp_eq_u32 s56, 10\n"
    "s_cselect_b32 s56, 2, s56\n"
    "s_lshl_b32 s50, s56, 19\n"
    "s_add_u32 s50, s50, s53\n"
    "s_lshl_b32 s51, s56, 7\n"
    "s_add_u32 s51, s51, s52\n"
    "s_add_u32 m0, s54, 40960\n"
    "s_nop 0\n"
    "buffer_load_dwordx4 v10, s[16:19], s50 offen offset:0 lds\n"
    "buffer_load_dwordx4 v11, s[16:19], s50 offen offset:1024 lds\n"
    "buffer_load_dwordx4 v12, s[16:19], s50 offen offset:2048 lds\n"
    "buffer_load_dwordx4 v13, s[16:19], s50 offen offset:3072 lds\n"
    "s_add_u32 m0, s55, 57344\n"
    "s_nop 0\n"
    "buffer_load_dwordx4 v4, s[12:15], s51 offen offset:0 lds\n"
    "buffer_load_dwordx4 v5, s[12:15], s51 offen offset:1024 lds\n"
    "buffer_load_dwordx4 v6, s[12:15], s51 offen offset:2048 lds\n"
    "buffer_load_dwordx4 v7, s[12:15], s51 offen offset:3072 lds\n"
    "s_add_u32 m0, s55, 61440\n"
    "s_nop 0\n"
    "buffer_load_dwordx4 v8, s[12:15], s51 offen offset:0 lds\n"
    "buffer_load_dwordx4 v9, s[12:15], s51 offen offset:1024 lds\n"
    "s_add_u32 s56, s56, 1\n"
    "s_cmp_eq_u32 s56, 10\n"
    "s_cselect_b32 s56, 2, s56\n"
    "s_lshl_b32 s50, s56, 19\n"
    "s_add_u32 s50, s50, s53\n"
    "s_lshl_b32 s51, s56, 7\n"
    "s_add_u32 s51, s51, s52\n"
    "s_add_u32 m0, s54, 0\n"
    "s_nop 0\n"
    "buffer_load_dwordx4 v10, s[16:19], s50 offen offset:0 lds\n"
    "buffer_load_dwordx4 v11, s[16:19], s50 offen offset:1024 lds\n"
    "buffer_load_dwordx4 v12, s[16:19], s50 offen offset:2048 lds\n"
    "buffer_load_dwordx4 v13, s[16:19], s50 offen offset:3072 lds\n"
    "s_add_u32 m0, s55, 16384\n"
    "s_nop 0\n"
    "buffer_load_dwordx4 v4, s[12:15], s51 offen offset:0 lds\n"
    "buffer_load_dwordx4 v5, s[12:15], s51 offen offset:1024 lds\n"
    "buffer_load_dwordx4 v6, s[12:15], s51 offen offset:2048 lds\n"
    "buffer_load_dwordx4 v7, s[12:15], s51 offen offset:3072 lds\n"
    "s_add_u32 m0, s55, 20480\n"
    "s_nop 0\n"
    "buffer_load_dwordx4 v8, s[12:15], s51 offen offset:0 lds\n"
    "buffer_load_dwordx4 v9, s[12:15], s51 offen offset:1024 lds\n"
    "s_waitcnt vmcnt(20)\n"
    "s_barrier\n"
    "s_add_u32 s56, s56, 1\n"
    "s_cmp_eq_u32 s56, 10\n"
    "s_cselect_b32 s56, 2, s56\n"
    "s_lshl_b32 s50, s56, 19\n"
    "s_add_u32 s50, s50, s53\n"
    "s_lshl_b32 s51, s56, 7\n"
    "s_add_u32 s51, s51, s52\n"
    "s_add_u32 m0, s54, 122880\n"
    "s_nop 0\n"
    "buffer_load_dwordx4 v10, s[16:19], s50 offen offset:0 lds\n"
    "buffer_load_dwordx4 v11, s[16:19], s50 offen offset:1024 lds\n"
    "buffer_load_dwordx4 v12, s[16:19], s50 offen offset:2048 lds\n"
    "buffer_load_dwordx4 v13, s[16:19], s50 offen offset:3072 lds\n"
    "s_add_u32 m0, s55, 139264\n"
    "s_nop 0\n"
    "buffer_load_dwordx4 v4, s[12:15], s51 offen offset:0 lds\n"
    "buffer_load_dwordx4 v5, s[12:15], s51 offen offset:1024 lds\n"
    "buffer_load_dwordx4 v6, s[12:15], s51 offen offset:2048 lds\n"
    "buffer_load_dwordx4 v7, s[12:15], s51 offen offset:3072 lds\n"
    "s_add_u32 m0, s55, 143360\n"
    "s_nop 0\n"
    "buffer_load_dwordx4 v8, s[12:15], s51 offen offset:0 lds\n"
    "buffer_load_dwordx4 v9, s[12:15], s51 offen offset:1024 lds\n"
    "s_add_u32 s50, s57, 4096\n"
    "buffer_load_dwordx4 v[32:35], v27, s[20:23], s57 offen offset:0 nt\n"
    "buffer_load_dwordx4 v[36:39], v27, s[20:23], s57 offen offset:1024 nt\n"
    "buffer_load_dwordx4 v[40:43], v27, s[20:23], s57 offen offset:2048 nt\n"
    "buffer_load_dwordx4 v[44:47], v27, s[20:23], s57 offen offset:3072 nt\n"
    "buffer_load_dwordx4 v[48:51], v27, s[20:23], s50 offen offset:0 nt\n"
    "buffer_load_dwordx4 v[52:55], v27, s[20:23], s50 offen offset:1024 nt\n"
    "s_waitcnt vmcnt(26)\n"
    "s_barrier\n"
    "s_add_u32 s56, s56, 1\n"
    "s_cmp_eq_u32 s56, 10\n"
    "s_cselect_b32 s56, 2, s56\n"
    "s_lshl_b32 s50, s56, 19\n"
    "s_add_u32 s50, s50, s53\n"
    "s_lshl_b32 s51, s56, 7\n"
    "s_add_u32 s51, s51, s52\n"
    "s_add_u32 m0, s54, 81920\n"
    "s_nop 0\n"
    "buffer_load_dwordx4 v10, s[16:19], s50 offen offset:0 lds\n"
    "buffer_load_dwordx4 v11, s[16:19], s50 offen offset:1024 lds\n"
    "buffer_load_dwordx4 v12, s[16:19], s50 offen offset:2048 lds\n"
    "buffer_load_dwordx4 v13, s[16:19], s50 offen offset:3072 lds\n"
    "s_add_u32 m0, s55, 98304\n"
    "s_nop 0\n"
    "buffer_load_dwordx4 v4, s[12:15], s51 offen offset:0 lds\n"
    "buffer_load_dwordx4 v5, s[12:15], s51 offen offset:1024 lds\n"
    "buffer_load_dwordx4 v6, s[12:15], s51 offen offset:2048 lds\n"
    "buffer_load_dwordx4 v7, s[12:15], s51 offen offset:3072 lds\n"
    "s_add_u32 m0, s55, 102400\n"
    "s_nop 0\n"
    "buffer_load_dwordx4 v8, s[12:15], s51 offen offset:0 lds\n"
    "buffer_load_dwordx4 v9, s[12:15], s51 offen offset:1024 lds\n"
    "s_waitcnt vmcnt(26)\n"
    "s_barrier\n"
    "s_add_u32 s56, s56, 1\n"
    "s_cmp_eq_u32 s56, 10\n"
    "s_cselect_b32 s56, 2, s56\n"
    "s_lshl_b32 s50, s56, 19\n"
    "s_add_u32 s50, s50, s53\n"
    "s_lshl_b32 s51, s56, 7\n"
    "s_add_u32 s51, s51, s52\n"
    "s_add_u32 m0, s54, 40960\n"
    "s_nop 0\n"
    "buffer_load_dwordx4 v10, s[16:19], s50 offen offset:0 lds\n"
    "buffer_load_dwordx4 v11, s[16:19], s50 offen offset:1024 lds\n"
    "buffer_load_dwordx4 v12, s[16:19], s50 offen offset:2048 lds\n"
    "buffer_load_dwordx4 v13, s[16:19], s50 offen offset:3072 lds\n"
    "s_add_u32 m0, s55, 57344\n"
    "s_nop 0\n"
    "buffer_load_dwordx4 v4, s[12:15], s51 offen offset:0 lds\n"
    "buffer_load_dwordx4 v5, s[12:15], s51 offen offset:1024 lds\n"
    "buffer_load_dwordx4 v6, s[12:15], s51 offen offset:2048 lds\n"
    "buffer_load_dwordx4 v7, s[12:15], s51 offen offset:3072 lds\n"
    "s_add_u32 m0, s55, 61440\n"
    "s_nop 0\n"
    "buffer_load_dwordx4 v8, s[12:15], s51 offen offset:0 lds\n"
    "buffer_load_dwordx4 v9, s[12:15], s51 offen offset:1024 lds\n"
    "s_add_u32 s50, s58, 4096\n"
    "buffer_load_dwordx4 v[56:59], v27, s[20:23], s58 offen offset:0 nt\n"
    "buffer_load_dwordx4 v[60:63], v27, s[20:23], s58 offen offset:1024 nt\n"
    "buffer_load_dwordx4 v[64:67], v27, s[20:23], s58 offen offset:2048 nt\n"
    "buffer_load_dwordx4 v[68:71], v27, s[20:23], s58 offen offset:3072 nt\n"
    "buffer_load_dwordx4 v[72:75], v27, s[20:23], s50 offen offset:0 nt\n"
    "buffer_load_dwordx4 v[76:79], v27, s[20:23], s50 offen offset:1024 nt\n"
    "s_waitcnt vmcnt(32)\n"
    "s_barrier\n"
    "s_add_u32 s56, s56, 1\n"
    "s_cmp_eq_u32 s56, 10\n"
    "s_cselect_b32 s56, 2, s56\n"
    "s_lshl_b32 s50, s56, 19\n"
    "s_add_u32 s50, s50, s53\n"
    "s_lshl_b32 s51, s56, 7\n"
    "s_add_u32 s51, s51, s52\n"
    "s_add_u32 m0, s54, 0\n"
    "s_nop 0\n"
    "buffer_load_dwordx4 v10, s[16:19], s50 offen offset:0 lds\n"
    "buffer_load_dwordx4 v11, s[16:19], s50 offen offset:1024 lds\n"
    "buffer_load_dwordx4 v12, s[16:19], s50 offen offset:2048 lds\n"
    "buffer_load_dwordx4 v13, s[16:19], s50 offen offset:3072 lds\n"
    "s_add_u32 m0, s55, 16384\n"
    "s_nop 0\n"
    "buffer_load_dwordx4 v4, s[12:15], s51 offen offset:0 lds\n"
    "buffer_load_dwordx4 v5, s[12:15], s51 offen offset:1024 lds\n"
    "buffer_load_dwordx4 v6, s[12:15], s51 offen offset:2048 lds\n"
    "buffer_load_dwordx4 v7, s[12:15], s51 offen offset:3072 lds\n"
    "s_add_u32 m0, s55, 20480\n"
    "s_nop 0\n"
    "buffer_load_dwordx4 v8, s[12:15], s51 offen offset:0 lds\n"
    "buffer_load_dwordx4 v9, s[12:15], s51 offen offset:1024 lds\n"
    "s_waitcnt vmcnt(26)\n"
    "s_barrier\n"
    "s_add_u32 m0, s55, 139008\n"
    "s_add_u32 s50, s58, 4096\n"
    "buffer_load_dwordx4 v14, s[24:27], s58 offen offset:0 nt lds\n"
    "buffer_load_dwordx4 v15, s[24:27], s58 offen offset:1024 nt lds\n"
    "buffer_load_dwordx4 v14, s[24:27], s58 offen offset:2048 nt lds\n"
    "buffer_load_dwordx4 v15, s[24:27], s58 offen offset:3072 nt lds\n"
    "s_add_u32 m0, s55, 143104\n"
    "s_nop 0\n"
    "buffer_load_dwordx4 v14, s[24:27], s50 offen offset:0 nt lds\n"
    "buffer_load_dwordx4 v15, s[24:27], s50 offen offset:1024 nt lds\n"
    "s_waitcnt vmcnt(22)\n"
    "s_barrier\n"
    "s_waitcnt vmcnt(42)\n"
    "ds_write_b128 v21, v[32:35] offset:24576\n"
    "ds_write_b128 v22, v[36:39] offset:24576\n"
    "ds_write_b128 v23, v[40:43] offset:24576\n"
    "ds_write_b128 v24, v[44:47] offset:24576\n"
    "ds_write_b128 v25, v[48:51] offset:24576\n"
    "ds_write_b128 v26, v[52:55] offset:24576\n"
    "s_add_u32 m0, s55, 114432\n"
    "s_add_u32 s50, s57, 4096\n"
    "buffer_load_dwordx4 v14, s[24:27], s57 offen offset:0 nt lds\n"
    "buffer_load_dwordx4 v15, s[24:27], s57 offen offset:1024 nt lds\n"
    "buffer_load_dwordx4 v14, s[24:27], s57 offen offset:2048 nt lds\n"
    "buffer_load_dwordx4 v15, s[24:27], s57 offen offset:3072 nt lds\n"
    "s_add_u32 m0, s55, 118528\n"
    "s_nop 0\n"
    "buffer_load_dwordx4 v14, s[24:27], s50 offen offset:0 nt lds\n"
    "buffer_load_dwordx4 v15, s[24:27], s50 offen offset:1024 nt lds\n"
    "s_waitcnt vmcnt(12)\n"
    "s_barrier\n"
    "s_waitcnt vmcnt(22)\n"
    "ds_write_b128 v21, v[56:59] offset:0\n"
    "ds_write_b128 v22, v[60:63] offset:0\n"
    "ds_write_b128 v23, v[64:67] offset:0\n"
    "ds_write_b128 v24, v[68:71] offset:0\n"
    "ds_write_b128 v25, v[72:75] offset:0\n"
    "ds_write_b128 v26, v[76:79] offset:0\n"
    "s_waitcnt lgkmcnt(0)\n"
    "s_barrier\n"
    ".Lfz_varend:\n"
    "s_add_u32 s36, s36, 1\n"
    "s_addc_u32 s37, s37, 0\n"
    "s_add_u32 s38, s38, 1\n"
    "s_addc_u32 s39, s39, 0\n"
    "s_add_u32 s50, s7, 0\n"
    "s_and_b32 s50, s50, 7\n"
    "s_mul_i32 s50, s50, 163840\n"
    "s_add_u32 s50, s50, s88\n"
    "s_add_u32 m0, s89, 0\n"
    "s_nop 0\n"
    "buffer_load_dwordx4 v16, s[28:31], s50 offen offset:0 lds\n"
    "buffer_load_dwordx4 v17, s[28:31], s50 offen offset:1024 lds\n"
    "buffer_load_dwordx4 v18, s[28:31], s50 offen offset:2048 lds\n"
    "buffer_load_dwordx4 v19, s[28:31], s50 offen offset:3072 lds\n"
    "s_add_u32 m0, s89, 4096\n"
    "s_nop 0\n"
    "buffer_load_dwordx4 v20, s[28:31], s50 offen lds\n"
    "s_waitcnt vmcnt(5)\n"
    "s_add_u32 s50, s7, 1\n"
    "s_and_b32 s50, s50, 7\n"
    "s_mul_i32 s50, s50, 163840\n"
    "s_add_u32 s50, s50, s88\n"
    "s_add_u32 m0, s89, 20480\n"
    "s_nop 0\n"
    "buffer_load_dwordx4 v16, s[28:31], s50 offen offset:0 lds\n"
    "buffer_load_dwordx4 v17, s[28:31], s50 offen offset:1024 lds\n"
    "buffer_load_dwordx4 v18, s[28:31], s50 offen offset:2048 lds\n"
    "buffer_load_dwordx4 v19, s[28:31], s50 offen offset:3072 lds\n"
    "s_add_u32 m0, s89, 24576\n"
    "s_nop 0\n"
    "buffer_load_dwordx4 v20, s[28:31], s50 offen lds\n"
    "s_barrier\n"
    "s_add_u32 s50, s7, 2\n"
    "s_and_b32 s50, s50, 7\n"
    "s_mul_i32 s50, s50, 163840\n"
    "s_add_u32 s50, s50, s88\n"
    "s_add_u32 m0, s89, 40960\n"
    "s_nop 0\n"
    "buffer_load_dwordx4 v16, s[28:31], s50 offen offset:0 lds\n"
    "buffer_load_dwordx4 v17, s[28:31], s50 offen offset:1024 lds\n"
    "buffer_load_dwordx4 v18, s[28:31], s50 offen offset:2048 lds\n"
    "buffer_load_dwordx4 v19, s[28:31], s50 offen offset:3072 lds\n"
    "s_add_u32 m0, s89, 45056\n"
    "s_nop 0\n"
    "buffer_load_dwordx4 v20, s[28:31], s50 offen lds\n"
    "s_waitcnt vmcnt(0)\n"
    "s_barrier\n"
    "s_add_u32 s50, s7, 3\n"
    "s_and_b32 s50, s50, 7\n"
    "s_mul_i32 s50, s50, 163840\n"
    "s_add_u32 s50, s50, s88\n"
    "s_add_u32 m0, s89, 61440\n"
    "s_nop 0\n"
    "buffer_load_dwordx4 v16, s[28:31], s50 offen offset:0 lds\n"
    "buffer_load_dwordx4 v17, s[28:31], s50 offen offset:1024 lds\n"
    "buffer_load_dwordx4 v18, s[28:31], s50 offen offset:2048 lds\n"
    "buffer_load_dwordx4 v19, s[28:31], s50 offen offset:3072 lds\n"
    "s_add_u32 m0, s89, 65536\n"
    "s_nop 0\n"
    "buffer_load_dwordx4 v20, s[28:31], s50 offen lds\n"
    "s_add_u32 s50, s7, 4\n"
    "s_and_b32 s50, s50, 7\n"
    "s_mul_i32 s50, s50, 163840\n"
    "s_add_u32 s50, s50, s88\n"
    "s_add_u32 m0, s89, 81920\n"
    "s_nop 0\n"
    "buffer_load_dwordx4 v16, s[28:31], s50 offen offset:0 lds\n"
    "buffer_load_dwordx4 v17, s[28:31], s50 offen offset:1024 lds\n"
    "buffer_load_dwordx4 v18, s[28:31], s50 offen offset:2048 lds\n"
    "buffer_load_dwordx4 v19, s[28:31], s50 offen offset:3072 lds\n"
    "s_add_u32 m0, s89, 86016\n"
    "s_nop 0\n"
    "buffer_load_dwordx4 v20, s[28:31], s50 offen lds\n"
    "s_add_u32 s50, s7, 5\n"
    "s_and_b32 s50, s50, 7\n"
    "s_mul_i32 s50, s50, 163840\n"
    "s_add_u32 s50, s50, s88\n"
    "s_add_u32 m0, s89, 102400\n"
    "s_nop 0\n"
    "buffer_load_dwordx4 v16, s[28:31], s50 offen offset:0 lds\n"
    "buffer_load_dwordx4 v17, s[28:31], s50 offen offset:1024 lds\n"
    "buffer_load_dwordx4 v18, s[28:31], s50 offen offset:2048 lds\n"
    "buffer_load_dwordx4 v19, s[28:31], s50 offen offset:3072 lds\n"
    "s_add_u32 m0, s89, 106496\n"
    "s_nop 0\n"
    "buffer_load_dwordx4 v20, s[28:31], s50 offen lds\n"
    "s_add_u32 s50, s7, 6\n"
    "s_and_b32 s50, s50, 7\n"
    "s_mul_i32 s50, s50, 163840\n"
    "s_add_u32 s50, s50, s88\n"
    "s_add_u32 m0, s89, 122880\n"
    "s_nop 0\n"
    "buffer_load_dwordx4 v16, s[28:31], s50 offen offset:0 lds\n"
    "buffer_load_dwordx4 v17, s[28:31], s50 offen offset:1024 lds\n"
    "buffer_load_dwordx4 v18, s[28:31], s50 offen offset:2048 lds\n"
    "buffer_load_dwordx4 v19, s[28:31], s50 offen offset:3072 lds\n"
    "s_add_u32 m0, s89, 126976\n"
    "s_nop 0\n"
    "buffer_load_dwordx4 v20, s[28:31], s50 offen lds\n"
    "s_add_u32 s50, s7, 7\n"
    "s_and_b32 s50, s50, 7\n"
    "s_mul_i32 s50, s50, 163840\n"
    "s_add_u32 s50, s50, s88\n"
    "s_add_u32 m0, s89, 143360\n"
    "s_nop 0\n"
    "buffer_load_dwordx4 v16, s[28:31], s50 offen offset:0 lds\n"
    "buffer_load_dwordx4 v17, s[28:31], s50 offen offset:1024 lds\n"
    "buffer_load_dwordx4 v18, s[28:31], s50 offen offset:2048 lds\n"
    "buffer_load_dwordx4 v19, s[28:31], s50 offen offset:3072 lds\n"
    "s_add_u32 m0, s89, 147456\n"
    "s_nop 0\n"
    "buffer_load_dwordx4 v20, s[28:31], s50 offen lds\n"
    "s_mov_b32 s50, 131072\n"
    ".Lfz_poll1:\n"
    "global_load_dwordx2 v[80:81], v82, s[32:33] sc1\n"
    "s_waitcnt vmcnt(0)\n"
    "v_cmp_ne_u64 vcc, s[36:37], v[80:81]\n"
    "s_cbranch_vccz .Lfz_poll1_done\n"
    "s_sleep 8\n"
    "s_sub_u32 s50, s50, 1\n"
    "s_cbranch_scc0 .Lfz_poll1\n"
    ".Lfz_poll1_done:\n"
    "s_barrier\n"
    "s_cmp_lg_u32 s7, 0\n"
    "s_cbranch_scc1 .Lfz_non1\n"
    "v_mov_b32 v80, s36\n"
    "v_mov_b32 v81, s37\n"
    "v_mov_b32 v82, s90\n"
    "s_mov_b64 exec, 1\n"
    "global_store_dwordx2 v82, v[80:81], s[32:33] sc1\n"
    "s_mov_b64 exec, -1\n"
    ".Lfz_non1:\n"
    "s_branch .Lfz_end\n"
    ".Lfz_compute:\n"
    "s_lshl_b32 s50, s5, 2\n"
    "s_or_b32 s51, s50, s6\n"
    "s_lshl_b32 s51, s51, 2\n"
    "s_or_b32 s51, s51, s8\n"
    "s_lshl_b32 s51, s51, 2\n"
    "s_or_b32 s51, s51, s4\n"
    "s_lshl_b32 s51, s51, 7\n"
    "s_add_u32 s51, s51, 20709376\n"
    "s_or_b32 s52, s50, s8\n"
    "s_lshl_b32 s53, s52, 3\n"
    "s_or_b32 s53, s53, s9\n"
    "s_lshl_b32 s53, s53, 2\n"
    "s_or_b32 s53, s53, s4\n"
    "s_lshl_b32 s90, s53, 7\n"
    "s_add_u32 s90, s90, 20725760\n"
    "s_waitcnt lgkmcnt(0)\n"
    "s_load_dwordx2 s[36:37], s[32:33], s51 glc\n"
    "s_load_dwordx2 s[38:39], s[32:33], s90 glc\n"
    "v_lshrrev_b32 v4, 2, v2\n"
    "v_and_b32 v5, 3, v1\n"
    "v_and_b32 v7, 1, v3\n"
    "v_lshl_or_b32 v6, v7, 2, v4\n"
    "v_lshrrev_b32 v20, 1, v5\n"
    "v_lshlrev_b32 v21, 1, v6\n"
    "v_xor_b32 v20, v20, v21\n"
    "v_lshlrev_b32 v20, 4, v20\n"
    "v_and_b32 v21, 1, v5\n"
    "v_lshl_or_b32 v20, v21, 3, v20\n"
    "v_lshl_add_u32 v21, v3, 3, v4\n"
    "v_lshl_or_b32 v8, v21, 8, v20\n"
    "v_add_u32 v8, 40960, v8\n"
    "v_xor_b32 v9, 32, v8\n"
    "v_xor_b32 v10, 64, v8\n"
    "v_xor_b32 v11, 96, v8\n"
    "v_xor_b32 v12, 128, v8\n"
    "v_xor_b32 v13, 160, v8\n"
    "v_xor_b32 v14, 192, v8\n"
    "v_xor_b32 v15, 224, v8\n"
    "v_lshl_add_u32 v21, v3, 2, v4\n"
    "v_lshl_or_b32 v19, v21, 8, v20\n"
    "s_cmp_lt_u32 s4, 2\n"
    "s_cselect_b32 s54, 0, 24576\n"
    "s_cselect_b32 s55, 24576, 0\n"
    "s_add_u32 s54, s54, 114432\n"
    "s_add_u32 s55, s55, 65280\n"
    "v_add_u32 v19, s54, v19\n"
    "v_lshrrev_b32 v20, 1, v3\n"
    "v_xor_b32 v20, v20, v2\n"
    "v_lshlrev_b32 v20, 4, v20\n"
    "v_lshl_or_b32 v20, v7, 3, v20\n"
    "v_lshl_or_b32 v18, v2, 8, v20\n"
    "v_add_u32 v18, s55, v18\n"
    "s_mul_i32 s54, s4, 6144\n"
    "s_add_u32 s54, s54, 57344\n"
    "v_lshrrev_b32 v20, 1, v2\n"
    "v_xor_b32 v20, v20, v3\n"
    "v_lshlrev_b32 v20, 4, v20\n"
    "v_lshl_or_b32 v20, v2, 7, v20\n"
    "v_add_u32 v16, s54, v20\n"
    "v_xor_b32 v17, 64, v16\n"
    "v_mov_b32 v160, 0\n"
    "v_mov_b32 v161, 0\n"
    "v_mov_b32 v162, 0\n"
    "v_mov_b32 v163, 0\n"
    "v_mov_b32 v164, 0\n"
    "v_mov_b32 v165, 0\n"
    "v_mov_b32 v166, 0\n"
    "v_mov_b32 v167, 0\n"
    "v_mov_b32 v168, 0\n"
    "v_mov_b32 v169, 0\n"
    "v_mov_b32 v170, 0\n"
    "v_mov_b32 v171, 0\n"
    "v_mov_b32 v172, 0\n"
    "v_mov_b32 v173, 0\n"
    "v_mov_b32 v174, 0\n"
    "v_mov_b32 v175, 0\n"
    "v_mov_b32 v176, 0\n"
    "v_mov_b32 v177, 0\n"
    "v_mov_b32 v178, 0\n"
    "v_mov_b32 v179, 0\n"
    "v_mov_b32 v180, 0\n"
    "v_mov_b32 v181, 0\n"
    "v_mov_b32 v182, 0\n"
    "v_mov_b32 v183, 0\n"
    "v_mov_b32 v184, 0\n"
    "v_mov_b32 v185, 0\n"
    "v_mov_b32 v186, 0\n"
    "v_mov_b32 v187, 0\n"
    "v_mov_b32 v188, 0\n"
    "v_mov_b32 v189, 0\n"
    "v_mov_b32 v190, 0\n"
    "v_mov_b32 v191, 0\n"
    "v_mov_b32 v192, 0\n"
    "v_mov_b32 v193, 0\n"
    "v_mov_b32 v194, 0\n"
    "v_mov_b32 v195, 0\n"
    "v_mov_b32 v196, 0\n"
    "v_mov_b32 v197, 0\n"
    "v_mov_b32 v198, 0\n"
    "v_mov_b32 v199, 0\n"
    "v_mov_b32 v200, 0\n"
    "v_mov_b32 v201, 0\n"
    "v_mov_b32 v202, 0\n"
    "v_mov_b32 v203, 0\n"
    "v_mov_b32 v204, 0\n"
    "v_mov_b32 v205, 0\n"
    "v_mov_b32 v206, 0\n"
    "v_mov_b32 v207, 0\n"
    "v_mov_b32 v208, 0\n"
    "v_mov_b32 v209, 0\n"
    "v_mov_b32 v210, 0\n"
    "v_mov_b32 v211, 0\n"
    "v_mov_b32 v212, 0\n"
    "v_mov_b32 v213, 0\n"
    "v_mov_b32 v214, 0\n"
    "v_mov_b32 v215, 0\n"
    "v_mov_b32 v216, 0\n"
    "v_mov_b32 v217, 0\n"
    "v_mov_b32 v218, 0\n"
    "v_mov_b32 v219, 0\n"
    "v_mov_b32 v220, 0\n"
    "v_mov_b32 v221, 0\n"
    "v_mov_b32 v222, 0\n"
    "v_mov_b32 v223, 0\n"
    "v_mov_b32 v224, 0\n"
    "v_mov_b32 v225, 0\n"
    "v_mov_b32 v226, 0\n"
    "v_mov_b32 v227, 0\n"
    "v_mov_b32 v228, 0\n"
    "v_mov_b32 v229, 0\n"
    "v_mov_b32 v230, 0\n"
    "v_mov_b32 v231, 0\n"
    "v_mov_b32 v232, 0\n"
    "v_mov_b32 v233, 0\n"
    "v_mov_b32 v234, 0\n"
    "v_mov_b32 v235, 0\n"
    "v_mov_b32 v236, 0\n"
    "v_mov_b32 v237, 0\n"
    "v_mov_b32 v238, 0\n"
    "v_mov_b32 v239, 0\n"
    "v_mov_b32 v240, 0\n"
    "v_mov_b32 v241, 0\n"
    "v_mov_b32 v242, 0\n"
    "v_mov_b32 v243, 0\n"
    "v_mov_b32 v244, 0\n"
    "v_mov_b32 v245, 0\n"
    "v_mov_b32 v246, 0\n"
    "v_mov_b32 v247, 0\n"
    "v_mov_b32 v248, 0\n"
    "v_mov_b32 v249, 0\n"
    "v_mov_b32 v250, 0\n"
    "v_mov_b32 v251, 0\n"
    "v_mov_b32 v252, 0\n"
    "v_mov_b32 v253, 0\n"
    "v_mov_b32 v254, 0\n"
    "v_mov_b32 v255, 0\n"
    "s_lshl_b32 s52, s52, 2\n"
    "s_or_b32 s52, s52, s4\n"
    "s_mul_i32 s52, s52, 393216\n"
    "s_add_u32 s12, s32, s52\n"
    "s_addc_u32 s13, s33, 0\n"
    "s_and_b32 s13, s13, 0xffff\n"
    "s_mov_b32 s14, 393216\n"
    "s_mov_b32 s15, 0x20000\n"
    "s_mul_i32 s53, s53, 30720\n"
    "s_add_u32 s53, s53, 12582912\n"
    "s_add_u32 s16, s32, s53\n"
    "s_addc_u32 s17, s33, 0\n"
    "s_and_b32 s17, s17, 0xffff\n"
    "s_mov_b32 s18, 30720\n"
    "s_mov_b32 s19, 0x20000\n"
    "s_sub_u32 s91, s6, s4\n"
    "s_add_u32 s91, s91, 3\n"
    "s_and_b32 s91, s91, 3\n"
    "s_mul_i32 s52, s8, 192\n"
    "s_mul_i32 s53, s4, 48\n"
    "s_add_u32 s52, s52, s53\n"
    "s_lshl_b32 s53, s91, 4\n"
    "s_add_u32 s52, s52, s53\n"
    "s_mul_i32 s52, s52, 2560\n"
    "s_mul_i32 s53, s5, 1280\n"
    "s_add_u32 s52, s52, s53\n"
    "s_mul_i32 s53, s9, 160\n"
    "s_add_u32 s52, s52, s53\n"
    "s_add_u32 s20, s34, s52\n"
    "s_addc_u32 s21, s35, 0\n"
    "s_and_b32 s21, s21, 0xffff\n"
    "s_mov_b32 s22, 38560\n"
    "s_mov_b32 s23, 0x20000\n"
    "s_mov_b32 s92, 0x3e0293ee\n"
    "s_mov_b32 s56, 40960\n"
    "s_mov_b32 s59, -40960\n"
    "s_waitcnt lgkmcnt(0)\n"
    "s_add_u32 s36, s36, 1\n"
    "s_addc_u32 s37, s37, 0\n"
    "s_add_u32 s38, s38, 1\n"
    "s_addc_u32 s39, s39, 0\n"
    "s_waitcnt vmcnt(0)\n"
    "s_barrier\n"
    "ds_read_b128 v[84:87], v16 offset:0\n"
    "ds_read_b64_tr_b16 v[20:21], v8 offset:0\n"
    "ds_read_b64_tr_b16 v[22:23], v8 offset:1024\n"
    "ds_read_b128 v[88:91], v16 offset:2048\n"
    "ds_read_b64_tr_b16 v[24:25], v9 offset:0\n"
    "ds_read_b128 v[92:95], v16 offset:4096\n"
    "ds_read_b64_tr_b16 v[26:27], v9 offset:1024\n"
    "ds_read_b64_tr_b16 v[28:29], v10 offset:0\n"
    "ds_read_b64_tr_b16 v[30:31], v10 offset:1024\n"
    "ds_read_b64_tr_b16 v[32:33], v11 offset:0\n"
    "ds_read_b64_tr_b16 v[34:35], v11 offset:1024\n"
    "ds_read_b64_tr_b16 v[36:37], v12 offset:0\n"
    "ds_read_b64_tr_b16 v[38:39], v12 offset:1024\n"
    "ds_read_b64_tr_b16 v[40:41], v13 offset:0\n"
    "ds_read_b64_tr_b16 v[42:43], v13 offset:1024\n"
    "ds_read_b64_tr_b16 v[44:45], v14 offset:0\n"
    "ds_read_b64_tr_b16 v[46:47], v14 offset:1024\n"
    "ds_read_b64_tr_b16 v[48:49], v15 offset:0\n"
    "ds_read_b64_tr_b16 v[50:51], v15 offset:1024\n"
    "s_waitcnt lgkmcnt(0)\n"
    "s_mov_b32 s57, 8\n"
    "s_mov_b32 s57, 2\n"
    ".Lfz_qloop0:\n"
    "v_mfma_f32_16x16x32_bf16 v[160:163], v[20:23], v[84:87], v[160:163]\n"
    "s_nop 3\n"
    "ds_read_b128 v[96:99], v17 offset:0\n"
    "ds_read_b64_tr_b16 v[52:53], v8 offset:8192\n"
    "v_mfma_f32_16x16x32_bf16 v[164:167], v[20:23], v[88:91], v[164:167]\n"
    "s_nop 3\n"
    "ds_read_b64_tr_b16 v[54:55], v8 offset:9216\n"
    "v_mfma_f32_16x16x32_bf16 v[168:171], v[20:23], v[92:95], v[168:171]\n"
    "s_nop 3\n"
    "ds_read_b128 v[100:103], v17 offset:2048\n"
    "ds_read_b64_tr_b16 v[56:57], v9 offset:8192\n"
    "v_mfma_f32_16x16x32_bf16 v[172:175], v[24:27], v[84:87], v[172:175]\n"
    "s_nop 3\n"
    "ds_read_b128 v[104:107], v17 offset:4096\n"
    "v_mfma_f32_16x16x32_bf16 v[176:179], v[24:27], v[88:91], v[176:179]\n"
    "s_nop 3\n"
    "ds_read_b64_tr_b16 v[58:59], v9 offset:9216\n"
    "ds_read_b64_tr_b16 v[60:61], v10 offset:8192\n"
    "v_mfma_f32_16x16x32_bf16 v[180:183], v[24:27], v[92:95], v[180:183]\n"
    "s_nop 3\n"
    "ds_read_b64_tr_b16 v[62:63], v10 offset:9216\n"
    "v_mfma_f32_16x16x32_bf16 v[184:187], v[28:31], v[84:87], v[184:187]\n"
    "s_nop 3\n"
    "ds_read_b64_tr_b16 v[64:65], v11 offset:8192\n"
    "ds_read_b64_tr_b16 v[66:67], v11 offset:9216\n"
    "v_mfma_f32_16x16x32_bf16 v[188:191], v[28:31], v[88:91], v[188:191]\n"
    "s_nop 3\n"
    "ds_read_b64_tr_b16 v[68:69], v12 offset:8192\n"
    "v_mfma_f32_16x16x32_bf16 v[192:195], v[28:31], v[92:95], v[192:195]\n"
    "s_nop 3\n"
    "ds_read_b64_tr_b16 v[70:71], v12 offset:9216\n"
    "ds_read_b64_tr_b16 v[72:73], v13 offset:8192\n"
    "v_mfma_f32_16x16x32_bf16 v[196:199], v[32:35], v[84:87], v[196:199]\n"
    "s_nop 3\n"
    "ds_read_b64_tr_b16 v[74:75], v13 offset:9216\n"
    "v_mfma_f32_16x16x32_bf16 v[200:203], v[32:35], v[88:91], v[200:203]\n"
    "s_nop 3\n"
    "ds_read_b64_tr_b16 v[76:77], v14 offset:8192\n"
    "ds_read_b64_tr_b16 v[78:79], v14 offset:9216\n"
    "v_mfma_f32_16x16x32_bf16 v[204:207], v[32:35], v[92:95], v[204:207]\n"
    "s_nop 3\n"
    "ds_read_b64_tr_b16 v[80:81], v15 offset:8192\n"
    "v_mfma_f32_16x16x32_bf16 v[208:211], v[36:39], v[84:87], v[208:211]\n"
    "s_nop 3\n"
    "ds_read_b64_tr_b16 v[82:83], v15 offset:9216\n"
    "s_cmp_eq_u32 s56, 0\n"
    "v_mfma_f32_16x16x32_bf16 v[212:215], v[36:39], v[88:91], v[212:215]\n"
    "s_nop 3\n"
    "s_cselect_b32 s58, 122880, s59\n"
    "s_add_u32 s56, s56, s58\n"
    "v_mfma_f32_16x16x32_bf16 v[216:219], v[36:39], v[92:95], v[216:219]\n"
    "s_nop 3\n"
    "v_add_u32 v8, s58, v8\n"
    "v_add_u32 v9, s58, v9\n"
    "v_mfma_f32_16x16x32_bf16 v[220:223], v[40:43], v[84:87], v[220:223]\n"
    "s_nop 3\n"
    "v_add_u32 v10, s58, v10\n"
    "v_add_u32 v11, s58, v11\n"
    "v_mfma_f32_16x16x32_bf16 v[224:227], v[40:43], v[88:91], v[224:227]\n"
    "s_nop 3\n"
    "v_add_u32 v12, s58, v12\n"
    "v_add_u32 v13, s58, v13\n"
    "v_mfma_f32_16x16x32_bf16 v[228:231], v[40:43], v[92:95], v[228:231]\n"
    "s_nop 3\n"
    "v_add_u32 v14, s58, v14\n"
    "v_add_u32 v15, s58, v15\n"
    "v_mfma_f32_16x16x32_bf16 v[232:235], v[44:47], v[84:87], v[232:235]\n"
    "s_nop 3\n"
    "v_add_u32 v16, s58, v16\n"
    "v_add_u32 v17, s58, v17\n"
    "v_mfma_f32_16x16x32_bf16 v[236:239], v[44:47], v[88:91], v[236:239]\n"
    "s_nop 3\n"
    "v_mfma_f32_16x16x32_bf16 v[240:243], v[44:47], v[92:95], v[240:243]\n"
    "s_nop 3\n"
    "v_mfma_f32_16x16x32_bf16 v[244:247], v[48:51], v[84:87], v[244:247]\n"
    "s_nop 3\n"
    "v_mfma_f32_16x16x32_bf16 v[248:251], v[48:51], v[88:91], v[248:251]\n"
    "s_nop 3\n"
    "s_waitcnt lgkmcnt(0)\n"
    "s_barrier\n"
    "v_mfma_f32_16x16x32_bf16 v[252:255], v[48:51], v[92:95], v[252:255]\n"
    "s_nop 3\n"
    "v_mfma_f32_16x16x32_bf16 v[160:163], v[52:55], v[96:99], v[160:163]\n"
    "s_nop 3\n"
    "ds_read_b128 v[84:87], v16 offset:0\n"
    "v_mfma_f32_16x16x32_bf16 v[164:167], v[52:55], v[100:103], v[164:167]\n"
    "s_nop 3\n"
    "ds_read_b64_tr_b16 v[20:21], v8 offset:0\n"
    "ds_read_b64_tr_b16 v[22:23], v8 offset:1024\n"
    "v_mfma_f32_16x16x32_bf16 v[168:171], v[52:55], v[104:107], v[168:171]\n"
    "s_nop 3\n"
    "ds_read_b128 v[88:91], v16 offset:2048\n"
    "v_mfma_f32_16x16x32_bf16 v[172:175], v[56:59], v[96:99], v[172:175]\n"
    "s_nop 3\n"
    "ds_read_b64_tr_b16 v[24:25], v9 offset:0\n"
    "ds_read_b128 v[92:95], v16 offset:4096\n"
    "v_mfma_f32_16x16x32_bf16 v[176:179], v[56:59], v[100:103], v[176:179]\n"
    "s_nop 3\n"
    "ds_read_b64_tr_b16 v[26:27], v9 offset:1024\n"
    "v_mfma_f32_16x16x32_bf16 v[180:183], v[56:59], v[104:107], v[180:183]\n"
    "s_nop 3\n"
    "ds_read_b64_tr_b16 v[28:29], v10 offset:0\n"
    "ds_read_b64_tr_b16 v[30:31], v10 offset:1024\n"
    "v_mfma_f32_16x16x32_bf16 v[184:187], v[60:63], v[96:99], v[184:187]\n"
    "s_nop 3\n"
    "ds_read_b64_tr_b16 v[32:33], v11 offset:0\n"
    "v_mfma_f32_16x16x32_bf16 v[188:191], v[60:63], v[100:103], v[188:191]\n"
    "s_nop 3\n"
    "ds_read_b64_tr_b16 v[34:35], v11 offset:1024\n"
    "ds_read_b64_tr_b16 v[36:37], v12 offset:0\n"
    "v_mfma_f32_16x16x32_bf16 v[192:195], v[60:63], v[104:107], v[192:195]\n"
    "s_nop 3\n"
    "ds_read_b64_tr_b16 v[38:39], v12 offset:1024\n"
    "v_mfma_f32_16x16x32_bf16 v[196:199], v[64:67], v[96:99], v[196:199]\n"
    "s_nop 3\n"
    "ds_read_b64_tr_b16 v[40:41], v13 offset:0\n"
    "ds_read_b64_tr_b16 v[42:43], v13 offset:1024\n"
    "v_mfma_f32_16x16x32_bf16 v[200:203], v[64:67], v[100:103], v[200:203]\n"
    "s_nop 3\n"
    "ds_read_b64_tr_b16 v[44:45], v14 offset:0\n"
    "v_mfma_f32_16x16x32_bf16 v[204:207], v[64:67], v[104:107], v[204:207]\n"
    "s_nop 3\n"
    "ds_read_b64_tr_b16 v[46:47], v14 offset:1024\n"
    "ds_read_b64_tr_b16 v[48:49], v15 offset:0\n"
    "v_mfma_f32_16x16x32_bf16 v[208:211], v[68:71], v[96:99], v[208:211]\n"
    "s_nop 3\n"
    "ds_read_b64_tr_b16 v[50:51], v15 offset:1024\n"
    "v_mfma_f32_16x16x32_bf16 v[212:215], v[68:71], v[100:103], v[212:215]\n"
    "s_nop 3\n"
    "s_sub_u32 s57, s57, 1\n"
    "v_mfma_f32_16x16x32_bf16 v[216:219], v[68:71], v[104:107], v[216:219]\n"
    "s_nop 3\n"
    "v_mfma_f32_16x16x32_bf16 v[220:223], v[72:75], v[96:99], v[220:223]\n"
    "s_nop 3\n"
    "v_mfma_f32_16x16x32_bf16 v[224:227], v[72:75], v[100:103], v[224:227]\n"
    "s_nop 3\n"
    "v_mfma_f32_16x16x32_bf16 v[228:231], v[72:75], v[104:107], v[228:231]\n"
    "s_nop 3\n"
    "v_mfma_f32_16x16x32_bf16 v[232:235], v[76:79], v[96:99], v[232:235]\n"
    "s_nop 3\n"
    "v_mfma_f32_16x16x32_bf16 v[236:239], v[76:79], v[100:103], v[236:239]\n"
    "s_nop 3\n"
    "v_mfma_f32_16x16x32_bf16 v[240:243], v[76:79], v[104:107], v[240:243]\n"
    "s_nop 3\n"
    "v_mfma_f32_16x16x32_bf16 v[244:247], v[80:83], v[96:99], v[244:247]\n"
    "s_nop 3\n"
    "v_mfma_f32_16x16x32_bf16 v[248:251], v[80:83], v[100:103], v[248:251]\n"
    "s_nop 3\n"
    "v_mfma_f32_16x16x32_bf16 v[252:255], v[80:83], v[104:107], v[252:255]\n"
    "s_nop 3\n"
    "s_waitcnt lgkmcnt(0)\n"
    "s_cbranch_scc0 .Lfz_qloop0\n"
    "s_mov_b32 s57, 2\n"
    ".Lfz_qloop1:\n"
    "v_mfma_f32_16x16x32_bf16 v[160:163], v[20:23], v[84:87], v[160:163]\n"
    "s_nop 2\n"
    "ds_read_b128 v[96:99], v17 offset:0\n"
    "ds_read_b64_tr_b16 v[52:53], v8 offset:8192\n"
    "v_mfma_f32_16x16x32_bf16 v[164:167], v[20:23], v[88:91], v[164:167]\n"
    "s_nop 2\n"
    "ds_read_b64_tr_b16 v[54:55], v8 offset:9216\n"
    "v_mfma_f32_16x16x32_bf16 v[168:171], v[20:23], v[92:95], v[168:171]\n"
    "s_nop 2\n"
    "ds_read_b128 v[100:103], v17 offset:2048\n"
    "ds_read_b64_tr_b16 v[56:57], v9 offset:8192\n"
    "v_mfma_f32_16x16x32_bf16 v[172:175], v[24:27], v[84:87], v[172:175]\n"
    "s_nop 2\n"
    "ds_read_b128 v[104:107], v17 offset:4096\n"
    "v_mfma_f32_16x16x32_bf16 v[176:179], v[24:27], v[88:91], v[176:179]\n"
    "s_nop 2\n"
    "ds_read_b64_tr_b16 v[58:59], v9 offset:9216\n"
    "ds_read_b64_tr_b16 v[60:61], v10 offset:8192\n"
    "v_mfma_f32_16x16x32_bf16 v[180:183], v[24:27], v[92:95], v[180:183]\n"
    "s_nop 2\n"
    "ds_read_b64_tr_b16 v[62:63], v10 offset:9216\n"
    "v_mfma_f32_16x16x32_bf16 v[184:187], v[28:31], v[84:87], v[184:187]\n"
    "s_nop 2\n"
    "ds_read_b64_tr_b16 v[64:65], v11 offset:8192\n"
    "ds_read_b64_tr_b16 v[66:67], v11 offset:9216\n"
    "v_mfma_f32_16x16x32_bf16 v[188:191], v[28:31], v[88:91], v[188:191]\n"
    "s_nop 2\n"
    "ds_read_b64_tr_b16 v[68:69], v12 offset:8192\n"
    "v_mfma_f32_16x16x32_bf16 v[192:195], v[28:31], v[92:95], v[192:195]\n"
    "s_nop 2\n"
    "ds_read_b64_tr_b16 v[70:71], v12 offset:9216\n"
    "ds_read_b64_tr_b16 v[72:73], v13 offset:8192\n"
    "v_mfma_f32_16x16x32_bf16 v[196:199], v[32:35], v[84:87], v[196:199]\n"
    "s_nop 2\n"
    "ds_read_b64_tr_b16 v[74:75], v13 offset:9216\n"
    "v_mfma_f32_16x16x32_bf16 v[200:203], v[32:35], v[88:91], v[200:203]\n"
    "s_nop 2\n"
    "ds_read_b64_tr_b16 v[76:77], v14 offset:8192\n"
    "ds_read_b64_tr_b16 v[78:79], v14 offset:9216\n"
    "v_mfma_f32_16x16x32_bf16 v[204:207], v[32:35], v[92:95], v[204:207]\n"
    "s_nop 2\n"
    "ds_read_b64_tr_b16 v[80:81], v15 offset:8192\n"
    "v_mfma_f32_16x16x32_bf16 v[208:211], v[36:39], v[84:87], v[208:211]\n"
    "s_nop 2\n"
    "ds_read_b64_tr_b16 v[82:83], v15 offset:9216\n"
    "s_cmp_eq_u32 s56, 0\n"
    "v_mfma_f32_16x16x32_bf16 v[212:215], v[36:39], v[88:91], v[212:215]\n"
    "s_nop 2\n"
    "s_cselect_b32 s58, 122880, s59\n"
    "s_add_u32 s56, s56, s58\n"
    "v_mfma_f32_16x16x32_bf16 v[216:219], v[36:39], v[92:95], v[216:219]\n"
    "s_nop 2\n"
    "v_add_u32 v8, s58, v8\n"
    "v_add_u32 v9, s58, v9\n"
    "v_mfma_f32_16x16x32_bf16 v[220:223], v[40:43], v[84:87], v[220:223]\n"
    "s_nop 2\n"
    "v_add_u32 v10, s58, v10\n"
    "v_add_u32 v11, s58, v11\n"
    "v_mfma_f32_16x16x32_bf16 v[224:227], v[40:43], v[88:91], v[224:227]\n"
    "s_nop 2\n"
    "v_add_u32 v12, s58, v12\n"
    "v_add_u32 v13, s58, v13\n"
    "v_mfma_f32_16x16x32_bf16 v[228:231], v[40:43], v[92:95], v[228:231]\n"
    "s_nop 2\n"
    "v_add_u32 v14, s58, v14\n"
    "v_add_u32 v15, s58, v15\n"
    "v_mfma_f32_16x16x32_bf16 v[232:235], v[44:47], v[84:87], v[232:235]\n"
    "s_nop 2\n"
    "v_add_u32 v16, s58, v16\n"
    "v_add_u32 v17, s58, v17\n"
    "v_mfma_f32_16x16x32_bf16 v[236:239], v[44:47], v[88:91], v[236:239]\n"
    "s_nop 2\n"
    "v_mfma_f32_16x16x32_bf16 v[240:243], v[44:47], v[92:95], v[240:243]\n"
    "s_nop 2\n"
    "v_mfma_f32_16x16x32_bf16 v[244:247], v[48:51], v[84:87], v[244:247]\n"
    "s_nop 2\n"
    "v_mfma_f32_16x16x32_bf16 v[248:251], v[48:51], v[88:91], v[248:251]\n"
    "s_nop 2\n"
    "s_waitcnt lgkmcnt(0)\n"
    "s_barrier\n"
    "v_mfma_f32_16x16x32_bf16 v[252:255], v[48:51], v[92:95], v[252:255]\n"
    "s_nop 2\n"
    "v_mfma_f32_16x16x32_bf16 v[160:163], v[52:55], v[96:99], v[160:163]\n"
    "s_nop 2\n"
    "ds_read_b128 v[84:87], v16 offset:0\n"
    "v_mfma_f32_16x16x32_bf16 v[164:167], v[52:55], v[100:103], v[164:167]\n"
    "s_nop 2\n"
    "ds_read_b64_tr_b16 v[20:21], v8 offset:0\n"
    "ds_read_b64_tr_b16 v[22:23], v8 offset:1024\n"
    "v_mfma_f32_16x16x32_bf16 v[168:171], v[52:55], v[104:107], v[168:171]\n"
    "s_nop 2\n"
    "ds_read_b128 v[88:91], v16 offset:2048\n"
    "v_mfma_f32_16x16x32_bf16 v[172:175], v[56:59], v[96:99], v[172:175]\n"
    "s_nop 2\n"
    "ds_read_b64_tr_b16 v[24:25], v9 offset:0\n"
    "ds_read_b128 v[92:95], v16 offset:4096\n"
    "v_mfma_f32_16x16x32_bf16 v[176:179], v[56:59], v[100:103], v[176:179]\n"
    "s_nop 2\n"
    "ds_read_b64_tr_b16 v[26:27], v9 offset:1024\n"
    "v_mfma_f32_16x16x32_bf16 v[180:183], v[56:59], v[104:107], v[180:183]\n"
    "s_nop 2\n"
    "ds_read_b64_tr_b16 v[28:29], v10 offset:0\n"
    "ds_read_b64_tr_b16 v[30:31], v10 offset:1024\n"
    "v_mfma_f32_16x16x32_bf16 v[184:187], v[60:63], v[96:99], v[184:187]\n"
    "s_nop 2\n"
    "ds_read_b64_tr_b16 v[32:33], v11 offset:0\n"
    "v_mfma_f32_16x16x32_bf16 v[188:191], v[60:63], v[100:103], v[188:191]\n"
    "s_nop 2\n"
    "ds_read_b64_tr_b16 v[34:35], v11 offset:1024\n"
    "ds_read_b64_tr_b16 v[36:37], v12 offset:0\n"
    "v_mfma_f32_16x16x32_bf16 v[192:195], v[60:63], v[104:107], v[192:195]\n"
    "s_nop 2\n"
    "ds_read_b64_tr_b16 v[38:39], v12 offset:1024\n"
    "v_mfma_f32_16x16x32_bf16 v[196:199], v[64:67], v[96:99], v[196:199]\n"
    "s_nop 2\n"
    "ds_read_b64_tr_b16 v[40:41], v13 offset:0\n"
    "ds_read_b64_tr_b16 v[42:43], v13 offset:1024\n"
    "v_mfma_f32_16x16x32_bf16 v[200:203], v[64:67], v[100:103], v[200:203]\n"
    "s_nop 2\n"
    "ds_read_b64_tr_b16 v[44:45], v14 offset:0\n"
    "v_mfma_f32_16x16x32_bf16 v[204:207], v[64:67], v[104:107], v[204:207]\n"
    "s_nop 2\n"
    "ds_read_b64_tr_b16 v[46:47], v14 offset:1024\n"
    "ds_read_b64_tr_b16 v[48:49], v15 offset:0\n"
    "v_mfma_f32_16x16x32_bf16 v[208:211], v[68:71], v[96:99], v[208:211]\n"
    "s_nop 2\n"
    "ds_read_b64_tr_b16 v[50:51], v15 offset:1024\n"
    "v_mfma_f32_16x16x32_bf16 v[212:215], v[68:71], v[100:103], v[212:215]\n"
    "s_nop 2\n"
    "s_sub_u32 s57, s57, 1\n"
    "v_mfma_f32_16x16x32_bf16 v[216:219], v[68:71], v[104:107], v[216:219]\n"
    "s_nop 2\n"
    "v_mfma_f32_16x16x32_bf16 v[220:223], v[72:75], v[96:99], v[220:223]\n"
    "s_nop 2\n"
    "v_mfma_f32_16x16x32_bf16 v[224:227], v[72:75], v[100:103], v[224:227]\n"
    "s_nop 2\n"
    "v_mfma_f32_16x16x32_bf16 v[228:231], v[72:75], v[104:107], v[228:231]\n"
    "s_nop 2\n"
    "v_mfma_f32_16x16x32_bf16 v[232:235], v[76:79], v[96:99], v[232:235]\n"
    "s_nop 2\n"
    "v_mfma_f32_16x16x32_bf16 v[236:239], v[76:79], v[100:103], v[236:239]\n"
    "s_nop 2\n"
    "v_mfma_f32_16x16x32_bf16 v[240:243], v[76:79], v[104:107], v[240:243]\n"
    "s_nop 2\n"
    "v_mfma_f32_16x16x32_bf16 v[244:247], v[80:83], v[96:99], v[244:247]\n"
    "s_nop 2\n"
    "v_mfma_f32_16x16x32_bf16 v[248:251], v[80:83], v[100:103], v[248:251]\n"
    "s_nop 2\n"
    "v_mfma_f32_16x16x32_bf16 v[252:255], v[80:83], v[104:107], v[252:255]\n"
    "s_nop 2\n"
    "s_waitcnt lgkmcnt(0)\n"
    "s_cbranch_scc0 .Lfz_qloop1\n"
    "s_mov_b32 s57, 2\n"
    ".Lfz_qloop2:\n"
    "v_mfma_f32_16x16x32_bf16 v[160:163], v[20:23], v[84:87], v[160:163]\n"
    "s_nop 1\n"
    "ds_read_b128 v[96:99], v17 offset:0\n"
    "ds_read_b64_tr_b16 v[52:53], v8 offset:8192\n"
    "v_mfma_f32_16x16x32_bf16 v[164:167], v[20:23], v[88:91], v[164:167]\n"
    "s_nop 1\n"
    "ds_read_b64_tr_b16 v[54:55], v8 offset:9216\n"
    "v_mfma_f32_16x16x32_bf16 v[168:171], v[20:23], v[92:95], v[168:171]\n"
    "s_nop 1\n"
    "ds_read_b128 v[100:103], v17 offset:2048\n"
    "ds_read_b64_tr_b16 v[56:57], v9 offset:8192\n"
    "v_mfma_f32_16x16x32_bf16 v[172:175], v[24:27], v[84:87], v[172:175]\n"
    "s_nop 1\n"
    "ds_read_b128 v[104:107], v17 offset:4096\n"
    "v_mfma_f32_16x16x32_bf16 v[176:179], v[24:27], v[88:91], v[176:179]\n"
    "s_nop 1\n"
    "ds_read_b64_tr_b16 v[58:59], v9 offset:9216\n"
    "ds_read_b64_tr_b16 v[60:61], v10 offset:8192\n"
    "v_mfma_f32_16x16x32_bf16 v[180:183], v[24:27], v[92:95], v[180:183]\n"
    "s_nop 1\n"
    "ds_read_b64_tr_b16 v[62:63], v10 offset:9216\n"
    "v_mfma_f32_16x16x32_bf16 v[184:187], v[28:31], v[84:87], v[184:187]\n"
    "s_nop 1\n"
    "ds_read_b64_tr_b16 v[64:65], v11 offset:8192\n"
    "ds_read_b64_tr_b16 v[66:67], v11 offset:9216\n"
    "v_mfma_f32_16x16x32_bf16 v[188:191], v[28:31], v[88:91], v[188:191]\n"
    "s_nop 1\n"
    "ds_read_b64_tr_b16 v[68:69], v12 offset:8192\n"
    "v_mfma_f32_16x16x32_bf16 v[192:195], v[28:31], v[92:95], v[192:195]\n"
    "s_nop 1\n"
    "ds_read_b64_tr_b16 v[70:71], v12 offset:9216\n"
    "ds_read_b64_tr_b16 v[72:73], v13 offset:8192\n"
    "v_mfma_f32_16x16x32_bf16 v[196:199], v[32:35], v[84:87], v[196:199]\n"
    "s_nop 1\n"
    "ds_read_b64_tr_b16 v[74:75], v13 offset:9216\n"
    "v_mfma_f32_16x16x32_bf16 v[200:203], v[32:35], v[88:91], v[200:203]\n"
    "s_nop 1\n"
    "ds_read_b64_tr_b16 v[76:77], v14 offset:8192\n"
    "ds_read_b64_tr_b16 v[78:79], v14 offset:9216\n"
    "v_mfma_f32_16x16x32_bf16 v[204:207], v[32:35], v[92:95], v[204:207]\n"
    "s_nop 1\n"
    "ds_read_b64_tr_b16 v[80:81], v15 offset:8192\n"
    "v_mfma_f32_16x16x32_bf16 v[208:211], v[36:39], v[84:87], v[208:211]\n"
    "s_nop 1\n"
    "ds_read_b64_tr_b16 v[82:83], v15 offset:9216\n"
    "s_cmp_eq_u32 s56, 0\n"
    "v_mfma_f32_16x16x32_bf16 v[212:215], v[36:39], v[88:91], v[212:215]\n"
    "s_nop 1\n"
    "s_cselect_b32 s58, 122880, s59\n"
    "s_add_u32 s56, s56, s58\n"
    "v_mfma_f32_16x16x32_bf16 v[216:219], v[36:39], v[92:95], v[216:219]\n"
    "s_nop 1\n"
    "v_add_u32 v8, s58, v8\n"
    "v_add_u32 v9, s58, v9\n"
    "v_mfma_f32_16x16x32_bf16 v[220:223], v[40:43], v[84:87], v[220:223]\n"
    "s_nop 1\n"
    "v_add_u32 v10, s58, v10\n"
    "v_add_u32 v11, s58, v11\n"
    "v_mfma_f32_16x16x32_bf16 v[224:227], v[40:43], v[88:91], v[224:227]\n"
    "s_nop 1\n"
    "v_add_u32 v12, s58, v12\n"
    "v_add_u32 v13, s58, v13\n"
    "v_mfma_f32_16x16x32_bf16 v[228:231], v[40:43], v[92:95], v[228:231]\n"
    "s_nop 1\n"
    "v_add_u32 v14, s58, v14\n"
    "v_add_u32 v15, s58, v15\n"
    "v_mfma_f32_16x16x32_bf16 v[232:235], v[44:47], v[84:87], v[232:235]\n"
    "s_nop 1\n"
    "v_add_u32 v16, s58, v16\n"
    "v_add_u32 v17, s58, v17\n"
    "v_mfma_f32_16x16x32_bf16 v[236:239], v[44:47], v[88:91], v[236:239]\n"
    "s_nop 1\n"
    "v_mfma_f32_16x16x32_bf16 v[240:243], v[44:47], v[92:95], v[240:243]\n"
    "s_nop 1\n"
    "v_mfma_f32_16x16x32_bf16 v[244:247], v[48:51], v[84:87], v[244:247]\n"
    "s_nop 1\n"
    "v_mfma_f32_16x16x32_bf16 v[248:251], v[48:51], v[88:91], v[248:251]\n"
    "s_nop 1\n"
    "s_waitcnt lgkmcnt(0)\n"
    "s_barrier\n"
    "v_mfma_f32_16x16x32_bf16 v[252:255], v[48:51], v[92:95], v[252:255]\n"
    "s_nop 1\n"
    "v_mfma_f32_16x16x32_bf16 v[160:163], v[52:55], v[96:99], v[160:163]\n"
    "s_nop 1\n"
    "ds_read_b128 v[84:87], v16 offset:0\n"
    "v_mfma_f32_16x16x32_bf16 v[164:167], v[52:55], v[100:103], v[164:167]\n"
    "s_nop 1\n"
    "ds_read_b64_tr_b16 v[20:21], v8 offset:0\n"
    "ds_read_b64_tr_b16 v[22:23], v8 offset:1024\n"
    "v_mfma_f32_16x16x32_bf16 v[168:171], v[52:55], v[104:107], v[168:171]\n"
    "s_nop 1\n"
    "ds_read_b128 v[88:91], v16 offset:2048\n"
    "v_mfma_f32_16x16x32_bf16 v[172:175], v[56:59], v[96:99], v[172:175]\n"
    "s_nop 1\n"
    "ds_read_b64_tr_b16 v[24:25], v9 offset:0\n"
    "ds_read_b128 v[92:95], v16 offset:4096\n"
    "v_mfma_f32_16x16x32_bf16 v[176:179], v[56:59], v[100:103], v[176:179]\n"
    "s_nop 1\n"
    "ds_read_b64_tr_b16 v[26:27], v9 offset:1024\n"
    "v_mfma_f32_16x16x32_bf16 v[180:183], v[56:59], v[104:107], v[180:183]\n"
    "s_nop 1\n"
    "ds_read_b64_tr_b16 v[28:29], v10 offset:0\n"
    "ds_read_b64_tr_b16 v[30:31], v10 offset:1024\n"
    "v_mfma_f32_16x16x32_bf16 v[184:187], v[60:63], v[96:99], v[184:187]\n"
    "s_nop 1\n"
    "ds_read_b64_tr_b16 v[32:33], v11 offset:0\n"
    "v_mfma_f32_16x16x32_bf16 v[188:191], v[60:63], v[100:103], v[188:191]\n"
    "s_nop 1\n"
    "ds_read_b64_tr_b16 v[34:35], v11 offset:1024\n"
    "ds_read_b64_tr_b16 v[36:37], v12 offset:0\n"
    "v_mfma_f32_16x16x32_bf16 v[192:195], v[60:63], v[104:107], v[192:195]\n"
    "s_nop 1\n"
    "ds_read_b64_tr_b16 v[38:39], v12 offset:1024\n"
    "v_mfma_f32_16x16x32_bf16 v[196:199], v[64:67], v[96:99], v[196:199]\n"
    "s_nop 1\n"
    "ds_read_b64_tr_b16 v[40:41], v13 offset:0\n"
    "ds_read_b64_tr_b16 v[42:43], v13 offset:1024\n"
    "v_mfma_f32_16x16x32_bf16 v[200:203], v[64:67], v[100:103], v[200:203]\n"
    "s_nop 1\n"
    "ds_read_b64_tr_b16 v[44:45], v14 offset:0\n"
    "v_mfma_f32_16x16x32_bf16 v[204:207], v[64:67], v[104:107], v[204:207]\n"
    "s_nop 1\n"
    "ds_read_b64_tr_b16 v[46:47], v14 offset:1024\n"
    "ds_read_b64_tr_b16 v[48:49], v15 offset:0\n"
    "v_mfma_f32_16x16x32_bf16 v[208:211], v[68:71], v[96:99], v[208:211]\n"
    "s_nop 1\n"
    "ds_read_b64_tr_b16 v[50:51], v15 offset:1024\n"
    "v_mfma_f32_16x16x32_bf16 v[212:215], v[68:71], v[100:103], v[212:215]\n"
    "s_nop 1\n"
    "s_sub_u32 s57, s57, 1\n"
    "v_mfma_f32_16x16x32_bf16 v[216:219], v[68:71], v[104:107], v[216:219]\n"
    "s_nop 1\n"
    "v_mfma_f32_16x16x32_bf16 v[220:223], v[72:75], v[96:99], v[220:223]\n"
    "s_nop 1\n"
    "v_mfma_f32_16x16x32_bf16 v[224:227], v[72:75], v[100:103], v[224:227]\n"
    "s_nop 1\n"
    "v_mfma_f32_16x16x32_bf16 v[228:231], v[72:75], v[104:107], v[228:231]\n"
    "s_nop 1\n"
    "v_mfma_f32_16x16x32_bf16 v[232:235], v[76:79], v[96:99], v[232:235]\n"
    "s_nop 1\n"
    "v_mfma_f32_16x16x32_bf16 v[236:239], v[76:79], v[100:103], v[236:239]\n"
    "s_nop 1\n"
    "v_mfma_f32_16x16x32_bf16 v[240:243], v[76:79], v[104:107], v[240:243]\n"
    "s_nop 1\n"
    "v_mfma_f32_16x16x32_bf16 v[244:247], v[80:83], v[96:99], v[244:247]\n"
    "s_nop 1\n"
    "v_mfma_f32_16x16x32_bf16 v[248:251], v[80:83], v[100:103], v[248:251]\n"
    "s_nop 1\n"
    "v_mfma_f32_16x16x32_bf16 v[252:255], v[80:83], v[104:107], v[252:255]\n"
    "s_nop 1\n"
    "s_waitcnt lgkmcnt(0)\n"
    "s_cbranch_scc0 .Lfz_qloop2\n"
    "v_mfma_f32_16x16x32_bf16 v[160:163], v[20:23], v[84:87], v[160:163]\n"
    "ds_read_b128 v[96:99], v17 offset:0\n"
    "ds_read_b64_tr_b16 v[52:53], v8 offset:8192\n"
    "v_mfma_f32_16x16x32_bf16 v[164:167], v[20:23], v[88:91], v[164:167]\n"
    "ds_read_b64_tr_b16 v[54:55], v8 offset:9216\n"
    "v_mfma_f32_16x16x32_bf16 v[168:171], v[20:23], v[92:95], v[168:171]\n"
    "ds_read_b128 v[100:103], v17 offset:2048\n"
    "ds_read_b64_tr_b16 v[56:57], v9 offset:8192\n"
    "v_mfma_f32_16x16x32_bf16 v[172:175], v[24:27], v[84:87], v[172:175]\n"
    "ds_read_b128 v[104:107], v17 offset:4096\n"
    "v_mfma_f32_16x16x32_bf16 v[176:179], v[24:27], v[88:91], v[176:179]\n"
    "ds_read_b64_tr_b16 v[58:59], v9 offset:9216\n"
    "ds_read_b64_tr_b16 v[60:61], v10 offset:8192\n"
    "v_mfma_f32_16x16x32_bf16 v[180:183], v[24:27], v[92:95], v[180:183]\n"
    "ds_read_b64_tr_b16 v[62:63], v10 offset:9216\n"
    "v_mfma_f32_16x16x32_bf16 v[184:187], v[28:31], v[84:87], v[184:187]\n"
    "ds_read_b64_tr_b16 v[64:65], v11 offset:8192\n"
    "ds_read_b64_tr_b16 v[66:67], v11 offset:9216\n"
    "v_mfma_f32_16x16x32_bf16 v[188:191], v[28:31], v[88:91], v[188:191]\n"
    "ds_read_b64_tr_b16 v[68:69], v12 offset:8192\n"
    "v_mfma_f32_16x16x32_bf16 v[192:195], v[28:31], v[92:95], v[192:195]\n"
    "ds_read_b64_tr_b16 v[70:71], v12 offset:9216\n"
    "ds_read_b64_tr_b16 v[72:73], v13 offset:8192\n"
    "v_mfma_f32_16x16x32_bf16 v[196:199], v[32:35], v[84:87], v[196:199]\n"
    "ds_read_b64_tr_b16 v[74:75], v13 offset:9216\n"
    "v_mfma_f32_16x16x32_bf16 v[200:203], v[32:35], v[88:91], v[200:203]\n"
    "ds_read_b64_tr_b16 v[76:77], v14 offset:8192\n"
    "ds_read_b64_tr_b16 v[78:79], v14 offset:9216\n"
    "v_mfma_f32_16x16x32_bf16 v[204:207], v[32:35], v[92:95], v[204:207]\n"
    "ds_read_b64_tr_b16 v[80:81], v15 offset:8192\n"
    "v_mfma_f32_16x16x32_bf16 v[208:211], v[36:39], v[84:87], v[208:211]\n"
    "ds_read_b64_tr_b16 v[82:83], v15 offset:9216\n"
    "v_xor_b32 v8, 0, v18\n"
    "v_mfma_f32_16x16x32_bf16 v[212:215], v[36:39], v[88:91], v[212:215]\n"
    "v_xor_b32 v9, 32, v18\n"
    "v_xor_b32 v10, 64, v18\n"
    "v_mfma_f32_16x16x32_bf16 v[216:219], v[36:39], v[92:95], v[216:219]\n"
    "v_xor_b32 v11, 96, v18\n"
    "v_xor_b32 v12, 128, v18\n"
    "v_mfma_f32_16x16x32_bf16 v[220:223], v[40:43], v[84:87], v[220:223]\n"
    "v_xor_b32 v13, 160, v18\n"
    "v_xor_b32 v14, 192, v18\n"
    "v_mfma_f32_16x16x32_bf16 v[224:227], v[40:43], v[88:91], v[224:227]\n"
    "v_xor_b32 v15, 224, v18\n"
    "v_mfma_f32_16x16x32_bf16 v[228:231], v[40:43], v[92:95], v[228:231]\n"
    "v_mfma_f32_16x16x32_bf16 v[232:235], v[44:47], v[84:87], v[232:235]\n"
    "v_mfma_f32_16x16x32_bf16 v[236:239], v[44:47], v[88:91], v[236:239]\n"
    "v_mfma_f32_16x16x32_bf16 v[240:243], v[44:47], v[92:95], v[240:243]\n"
    "v_mfma_f32_16x16x32_bf16 v[244:247], v[48:51], v[84:87], v[244:247]\n"
    "v_mfma_f32_16x16x32_bf16 v[248:251], v[48:51], v[88:91], v[248:251]\n"
    "s_waitcnt lgkmcnt(0)\n"
    "s_barrier\n"
    "v_mfma_f32_16x16x32_bf16 v[252:255], v[48:51], v[92:95], v[252:255]\n"
    "ds_read_b64 v[108:109], v8 offset:0\n"
    "ds_read_b64 v[110:111], v9 offset:0\n"
    "v_mfma_f32_16x16x32_bf16 v[160:163], v[52:55], v[96:99], v[160:163]\n"
    "ds_read_b64 v[112:113], v10 offset:0\n"
    "ds_read_b64 v[114:115], v11 offset:0\n"
    "v_mfma_f32_16x16x32_bf16 v[164:167], v[52:55], v[100:103], v[164:167]\n"
    "ds_read_b64 v[116:117], v12 offset:0\n"
    "ds_read_b64 v[118:119], v13 offset:0\n"
    "v_mfma_f32_16x16x32_bf16 v[168:171], v[52:55], v[104:107], v[168:171]\n"
    "ds_read_b64 v[120:121], v14 offset:0\n"
    "ds_read_b64 v[122:123], v15 offset:0\n"
    "v_mfma_f32_16x16x32_bf16 v[172:175], v[56:59], v[96:99], v[172:175]\n"
    "ds_read_b64 v[124:125], v8 offset:4096\n"
    "ds_read_b64 v[126:127], v9 offset:4096\n"
    "v_mfma_f32_16x16x32_bf16 v[176:179], v[56:59], v[100:103], v[176:179]\n"
    "ds_read_b64 v[128:129], v10 offset:4096\n"
    "ds_read_b64 v[130:131], v11 offset:4096\n"
    "v_mfma_f32_16x16x32_bf16 v[180:183], v[56:59], v[104:107], v[180:183]\n"
    "ds_read_b64 v[132:133], v12 offset:4096\n"
    "ds_read_b64 v[134:135], v13 offset:4096\n"
    "v_mfma_f32_16x16x32_bf16 v[184:187], v[60:63], v[96:99], v[184:187]\n"
    "ds_read_b64 v[136:137], v14 offset:4096\n"
    "ds_read_b64 v[138:139], v15 offset:4096\n"
    "v_mfma_f32_16x16x32_bf16 v[188:191], v[60:63], v[100:103], v[188:191]\n"
    "ds_read_b64 v[140:141], v8 offset:8192\n"
    "ds_read_b64 v[142:143], v9 offset:8192\n"
    "v_mfma_f32_16x16x32_bf16 v[192:195], v[60:63], v[104:107], v[192:195]\n"
    "ds_read_b64 v[144:145], v10 offset:8192\n"
    "ds_read_b64 v[146:147], v11 offset:8192\n"
    "v_mfma_f32_16x16x32_bf16 v[196:199], v[64:67], v[96:99], v[196:199]\n"
    "ds_read_b64 v[148:149], v12 offset:8192\n"
    "ds_read_b64 v[150:151], v13 offset:8192\n"
    "v_mfma_f32_16x16x32_bf16 v[200:203], v[64:67], v[100:103], v[200:203]\n"
    "ds_read_b64 v[152:153], v14 offset:8192\n"
    "ds_read_b64 v[154:155], v15 offset:8192\n"
    "v_mfma_f32_16x16x32_bf16 v[204:207], v[64:67], v[104:107], v[204:207]\n"
    "ds_read_b64 v[156:157], v8 offset:12288\n"
    "ds_read_b64 v[158:159], v9 offset:12288\n"
    "v_mfma_f32_16x16x32_bf16 v[208:211], v[68:71], v[96:99], v[208:211]\n"
    "ds_read_b64 v[20:21], v10 offset:12288\n"
    "ds_read_b64 v[22:23], v11 offset:12288\n"
    "v_mfma_f32_16x16x32_bf16 v[212:215], v[68:71], v[100:103], v[212:215]\n"
    "ds_read_b64 v[24:25], v12 offset:12288\n"
    "ds_read_b64 v[26:27], v13 offset:12288\n"
    "v_mfma_f32_16x16x32_bf16 v[216:219], v[68:71], v[104:107], v[216:219]\n"
    "ds_read_b64 v[28:29], v14 offset:12288\n"
    "ds_read_b64 v[30:31], v15 offset:12288\n"
    "v_mfma_f32_16x16x32_bf16 v[220:223], v[72:75], v[96:99], v[220:223]\n"
    "ds_read_b64 v[32:33], v8 offset:16384\n"
    "ds_read_b64 v[34:35], v9 offset:16384\n"
    "v_mfma_f32_16x16x32_bf16 v[224:227], v[72:75], v[100:103], v[224:227]\n"
    "ds_read_b64 v[36:37], v10 offset:16384\n"
    "ds_read_b64 v[38:39], v11 offset:16384\n"
    "v_mfma_f32_16x16x32_bf16 v[228:231], v[72:75], v[104:107], v[228:231]\n"
    "ds_read_b64 v[40:41], v12 offset:16384\n"
    "ds_read_b64 v[42:43], v13 offset:16384\n"
    "v_mfma_f32_16x16x32_bf16 v[232:235], v[76:79], v[96:99], v[232:235]\n"
    "ds_read_b64 v[44:45], v14 offset:16384\n"
    "ds_read_b64 v[46:47], v15 offset:16384\n"
    "v_mfma_f32_16x16x32_bf16 v[236:239], v[76:79], v[100:103], v[236:239]\n"
    "ds_read_b64 v[48:49], v8 offset:20480\n"
    "ds_read_b64 v[50:51], v9 offset:20480\n"
    "v_mfma_f32_16x16x32_bf16 v[240:243], v[76:79], v[104:107], v[240:243]\n"
    "ds_read_b64 v[84:85], v10 offset:20480\n"
    "ds_read_b64 v[86:87], v11 offset:20480\n"
    "v_mfma_f32_16x16x32_bf16 v[244:247], v[80:83], v[96:99], v[244:247]\n"
    "ds_read_b64 v[88:89], v12 offset:20480\n"
    "ds_read_b64 v[90:91], v13 offset:20480\n"
    "v_mfma_f32_16x16x32_bf16 v[248:251], v[80:83], v[100:103], v[248:251]\n"
    "ds_read_b64 v[92:93], v14 offset:20480\n"
    "ds_read_b64 v[94:95], v15 offset:20480\n"
    "v_mfma_f32_16x16x32_bf16 v[252:255], v[80:83], v[104:107], v[252:255]\n"
    "v_cvt_pk_bf16_f32 v160, v160, v161\n"
    "v_cvt_pk_bf16_f32 v161, v162, v163\n"
    "v_cvt_pk_bf16_f32 v162, v172, v173\n"
    "v_cvt_pk_bf16_f32 v163, v174, v175\n"
    "v_cvt_pk_bf16_f32 v184, v184, v185\n"
    "v_cvt_pk_bf16_f32 v185, v186, v187\n"
    "v_cvt_pk_bf16_f32 v186, v196, v197\n"
    "v_cvt_pk_bf16_f32 v187, v198, v199\n"
    "v_cvt_pk_bf16_f32 v208, v208, v209\n"
    "v_cvt_pk_bf16_f32 v209, v210, v211\n"
    "v_cvt_pk_bf16_f32 v210, v220, v221\n"
    "v_cvt_pk_bf16_f32 v211, v222, v223\n"
    "v_cvt_pk_bf16_f32 v232, v232, v233\n"
    "v_cvt_pk_bf16_f32 v233, v234, v235\n"
    "v_cvt_pk_bf16_f32 v234, v244, v245\n"
    "v_cvt_pk_bf16_f32 v235, v246, v247\n"
    "s_waitcnt lgkmcnt(15)\n"
    "v_mfma_f32_16x16x32_bf16 v[52:55], v[108:111], v[160:163], 0\n"
    "v_cvt_pk_bf16_f32 v164, v164, v165\n"
    "v_mfma_f32_16x16x32_bf16 v[52:55], v[112:115], v[184:187], v[52:55]\n"
    "v_cvt_pk_bf16_f32 v165, v166, v167\n"
    "v_cvt_pk_bf16_f32 v166, v176, v177\n"
    "v_mfma_f32_16x16x32_bf16 v[52:55], v[116:119], v[208:211], v[52:55]\n"
    "v_cvt_pk_bf16_f32 v167, v178, v179\n"
    "v_cvt_pk_bf16_f32 v188, v188, v189\n"
    "v_mfma_f32_16x16x32_bf16 v[52:55], v[120:123], v[232:235], v[52:55]\n"
    "v_cvt_pk_bf16_f32 v189, v190, v191\n"
    "v_mfma_f32_16x16x32_bf16 v[56:59], v[124:127], v[160:163], 0\n"
    "v_cvt_pk_bf16_f32 v190, v200, v201\n"
    "v_cvt_pk_bf16_f32 v191, v202, v203\n"
    "v_mfma_f32_16x16x32_bf16 v[56:59], v[128:131], v[184:187], v[56:59]\n"
    "v_cvt_pk_bf16_f32 v212, v212, v213\n"
    "v_cvt_pk_bf16_f32 v213, v214, v215\n"
    "v_mfma_f32_16x16x32_bf16 v[56:59], v[132:135], v[208:211], v[56:59]\n"
    "v_cvt_pk_bf16_f32 v214, v224, v225\n"
    "v_mfma_f32_16x16x32_bf16 v[56:59], v[136:139], v[232:235], v[56:59]\n"
    "v_cvt_pk_bf16_f32 v215, v226, v227\n"
    "v_cvt_pk_bf16_f32 v236, v236, v237\n"
    "v_mfma_f32_16x16x32_bf16 v[60:63], v[140:143], v[160:163], 0\n"
    "v_cvt_pk_bf16_f32 v237, v238, v239\n"
    "v_cvt_pk_bf16_f32 v238, v248, v249\n"
    "v_mfma_f32_16x16x32_bf16 v[60:63], v[144:147], v[184:187], v[60:63]\n"
    "v_cvt_pk_bf16_f32 v239, v250, v251\n"
    "v_mfma_f32_16x16x32_bf16 v[60:63], v[148:151], v[208:211], v[60:63]\n"
    "v_cvt_pk_bf16_f32 v168, v168, v169\n"
    "v_cvt_pk_bf16_f32 v169, v170, v171\n"
    "v_mfma_f32_16x16x32_bf16 v[60:63], v[152:155], v[232:235], v[60:63]\n"
    "v_cvt_pk_bf16_f32 v170, v180, v181\n"
    "v_cvt_pk_bf16_f32 v171, v182, v183\n"
    "v_mfma_f32_16x16x32_bf16 v[64:67], v[156:159], v[160:163], 0\n"
    "v_cvt_pk_bf16_f32 v192, v192, v193\n"
    "v_mfma_f32_16x16x32_bf16 v[64:67], v[20:23], v[184:187], v[64:67]\n"
    "v_cvt_pk_bf16_f32 v193, v194, v195\n"
    "v_cvt_pk_bf16_f32 v194, v204, v205\n"
    "v_mfma_f32_16x16x32_bf16 v[64:67], v[24:27], v[208:211], v[64:67]\n"
    "v_cvt_pk_bf16_f32 v195, v206, v207\n"
    "v_cvt_pk_bf16_f32 v216, v216, v217\n"
    "v_mfma_f32_16x16x32_bf16 v[64:67], v[28:31], v[232:235], v[64:67]\n"
    "v_cvt_pk_bf16_f32 v217, v218, v219\n"
    "s_waitcnt lgkmcnt(14)\n"
    "v_mfma_f32_16x16x32_bf16 v[68:71], v[32:35], v[160:163], 0\n"
    "v_cvt_pk_bf16_f32 v218, v228, v229\n"
    "v_cvt_pk_bf16_f32 v219, v230, v231\n"
    "s_waitcnt lgkmcnt(12)\n"
    "v_mfma_f32_16x16x32_bf16 v[68:71], v[36:39], v[184:187], v[68:71]\n"
    "v_cvt_pk_bf16_f32 v240, v240, v241\n"
    "v_cvt_pk_bf16_f32 v241, v242, v243\n"
    "s_waitcnt lgkmcnt(10)\n"
    "v_mfma_f32_16x16x32_bf16 v[68:71], v[40:43], v[208:211], v[68:71]\n"
    "v_cvt_pk_bf16_f32 v242, v252, v253\n"
    "s_waitcnt lgkmcnt(8)\n"
    "v_mfma_f32_16x16x32_bf16 v[68:71], v[44:47], v[232:235], v[68:71]\n"
    "v_cvt_pk_bf16_f32 v243, v254, v255\n"
    "s_waitcnt lgkmcnt(6)\n"
    "v_mfma_f32_16x16x32_bf16 v[72:75], v[48:51], v[160:163], 0\n"
    "s_waitcnt lgkmcnt(4)\n"
    "v_mfma_f32_16x16x32_bf16 v[72:75], v[84:87], v[184:187], v[72:75]\n"
    "s_waitcnt lgkmcnt(2)\n"
    "v_mfma_f32_16x16x32_bf16 v[72:75], v[88:91], v[208:211], v[72:75]\n"
    "s_waitcnt lgkmcnt(0)\n"
    "v_mfma_f32_16x16x32_bf16 v[72:75], v[92:95], v[232:235], v[72:75]\n"
    "v_mfma_f32_16x16x32_bf16 v[172:175], v[108:111], v[164:167], 0\n"
    "v_xor_b32 v8, 0, v19\n"
    "v_xor_b32 v9, 32, v19\n"
    "v_xor_b32 v10, 64, v19\n"
    "v_xor_b32 v11, 96, v19\n"
    "v_mfma_f32_16x16x32_bf16 v[172:175], v[112:115], v[188:191], v[172:175]\n"
    "v_xor_b32 v12, 128, v19\n"
    "v_xor_b32 v13, 160, v19\n"
    "v_xor_b32 v14, 192, v19\n"
    "v_xor_b32 v15, 224, v19\n"
    "v_mfma_f32_16x16x32_bf16 v[172:175], v[116:119], v[212:215], v[172:175]\n"
    "v_max3_f32 v76, v52, v53, v54\n"
    "v_max3_f32 v77, v55, v56, v57\n"
    "v_max3_f32 v76, v76, v58, v59\n"
    "v_max3_f32 v77, v77, v60, v61\n"
    "v_mfma_f32_16x16x32_bf16 v[172:175], v[120:123], v[236:239], v[172:175]\n"
    "v_max3_f32 v76, v76, v62, v63\n"
    "v_max3_f32 v77, v77, v64, v65\n"
    "v_max3_f32 v76, v76, v66, v67\n"
    "v_max3_f32 v77, v77, v68, v69\n"
    "v_mfma_f32_16x16x32_bf16 v[196:199], v[124:127], v[164:167], 0\n"
    "v_max3_f32 v76, v76, v70, v71\n"
    "v_max3_f32 v77, v77, v72, v73\n"
    "v_max3_f32 v76, v76, v74, v75\n"
    "v_max_f32 v76, v76, v77\n"
    "v_mfma_f32_16x16x32_bf16 v[196:199], v[128:131], v[188:191], v[196:199]\n"
    "v_mov_b32 v77, v76\n"
    "s_nop 1\n"
    "v_permlane16_swap_b32 v76, v77\n"
    "v_max_f32 v76, v76, v77\n"
    "v_mov_b32 v77, v76\n"
    "v_mfma_f32_16x16x32_bf16 v[196:199], v[132:135], v[212:215], v[196:199]\n"
    "s_nop 0\n"
    "v_permlane32_swap_b32 v76, v77\n"
    "v_max_f32 v76, v76, v77\n"
    "v_mul_f32 v76, s92, v76\n"
    "v_pk_fma_f32 v[52:53], v[52:53], s[92:93], v[76:77] op_sel_hi:[1,0,0] neg_lo:[0,0,1] neg_hi:[0,0,1]\n"
    "v_mfma_f32_16x16x32_bf16 v[196:199], v[136:139], v[236:239], v[196:199]\n"
    "v_pk_fma_f32 v[54:55], v[54:55], s[92:93], v[76:77] op_sel_hi:[1,0,0] neg_lo:[0,0,1] neg_hi:[0,0,1]\n"
    "v_exp_f32 v52, v52\n"
    "v_exp_f32 v53, v53\n"
    "v_pk_fma_f32 v[56:57], v[56:57], s[92:93], v[76:77] op_sel_hi:[1,0,0] neg_lo:[0,0,1] neg_hi:[0,0,1]\n"
    "v_mfma_f32_16x16x32_bf16 v[220:223], v[140:143], v[164:167], 0\n"
    "v_exp_f32 v54, v54\n"
    "v_exp_f32 v55, v55\n"
    "v_pk_fma_f32 v[58:59], v[58:59], s[92:93], v[76:77] op_sel_hi:[1,0,0] neg_lo:[0,0,1] neg_hi:[0,0,1]\n"
    "v_exp_f32 v56, v56\n"
    "v_mfma_f32_16x16x32_bf16 v[220:223], v[144:147], v[188:191], v[220:223]\n"
    "v_exp_f32 v57, v57\n"
    "v_pk_fma_f32 v[60:61], v[60:61], s[92:93], v[76:77] op_sel_hi:[1,0,0] neg_lo:[0,0,1] neg_hi:[0,0,1]\n"
    "v_exp_f32 v58, v58\n"
    "v_exp_f32 v59, v59\n"
    "v_mfma_f32_16x16x32_bf16 v[220:223], v[148:151], v[212:215], v[220:223]\n"
    "v_pk_fma_f32 v[62:63], v[62:63], s[92:93], v[76:77] op_sel_hi:[1,0,0] neg_lo:[0,0,1] neg_hi:[0,0,1]\n"
    "v_exp_f32 v60, v60\n"
    "v_exp_f32 v61, v61\n"
    "v_cvt_pk_bf16_f32 v52, v52, v53\n"
    "v_mfma_f32_16x16x32_bf16 v[220:223], v[152:155], v[236:239], v[220:223]\n"
    "v_cvt_pk_bf16_f32 v53, v54, v55\n"
    "v_cvt_pk_bf16_f32 v54, v56, v57\n"
    "v_cvt_pk_bf16_f32 v55, v58, v59\n"
    "v_pk_fma_f32 v[64:65], v[64:65], s[92:93], v[76:77] op_sel_hi:[1,0,0] neg_lo:[0,0,1] neg_hi:[0,0,1]\n"
    "v_mfma_f32_16x16x32_bf16 v[244:247], v[156:159], v[164:167], 0\n"
    "v_exp_f32 v62, v62\n"
    "v_exp_f32 v63, v63\n"
    "v_pk_fma_f32 v[66:67], v[66:67], s[92:93], v[76:77] op_sel_hi:[1,0,0] neg_lo:[0,0,1] neg_hi:[0,0,1]\n"
    "v_exp_f32 v64, v64\n"
    "v_mfma_f32_16x16x32_bf16 v[244:247], v[20:23], v[188:191], v[244:247]\n"
    "v_exp_f32 v65, v65\n"
    "v_pk_fma_f32 v[68:69], v[68:69], s[92:93], v[76:77] op_sel_hi:[1,0,0] neg_lo:[0,0,1] neg_hi:[0,0,1]\n"
    "v_exp_f32 v66, v66\n"
    "v_exp_f32 v67, v67\n"
    "v_mfma_f32_16x16x32_bf16 v[244:247], v[24:27], v[212:215], v[244:247]\n"
    "v_pk_fma_f32 v[70:71], v[70:71], s[92:93], v[76:77] op_sel_hi:[1,0,0] neg_lo:[0,0,1] neg_hi:[0,0,1]\n"
    "v_exp_f32 v68, v68\n"
    "v_exp_f32 v69, v69\n"
    "v_cvt_pk_bf16_f32 v60, v60, v61\n"
    "v_mfma_f32_16x16x32_bf16 v[244:247], v[28:31], v[236:239], v[244:247]\n"
    "v_cvt_pk_bf16_f32 v61, v62, v63\n"
    "v_cvt_pk_bf16_f32 v62, v64, v65\n"
    "v_cvt_pk_bf16_f32 v63, v66, v67\n"
    "v_pk_fma_f32 v[72:73], v[72:73], s[92:93], v[76:77] op_sel_hi:[1,0,0] neg_lo:[0,0,1] neg_hi:[0,0,1]\n"
    "v_mfma_f32_16x16x32_bf16 v[176:179], v[32:35], v[164:167], 0\n"
    "v_exp_f32 v70, v70\n"
    "v_exp_f32 v71, v71\n"
    "v_pk_fma_f32 v[74:75], v[74:75], s[92:93], v[76:77] op_sel_hi:[1,0,0] neg_lo:[0,0,1] neg_hi:[0,0,1]\n"
    "v_exp_f32 v72, v72\n"
    "v_mfma_f32_16x16x32_bf16 v[176:179], v[36:39], v[188:191], v[176:179]\n"
    "v_exp_f32 v73, v73\n"
    "v_exp_f32 v74, v74\n"
    "v_exp_f32 v75, v75\n"
    "v_cvt_pk_bf16_f32 v68, v68, v69\n"
    "v_mfma_f32_16x16x32_bf16 v[176:179], v[40:43], v[212:215], v[176:179]\n"
    "v_cvt_pk_bf16_f32 v69, v70, v71\n"
    "v_cvt_pk_bf16_f32 v70, v72, v73\n"
    "v_cvt_pk_bf16_f32 v71, v74, v75\n"
    "v_mfma_f32_16x16x32_bf16 v[176:179], v[44:47], v[236:239], v[176:179]\n"
    "v_mfma_f32_16x16x32_bf16 v[200:203], v[48:51], v[164:167], 0\n"
    "v_mfma_f32_16x16x32_bf16 v[200:203], v[84:87], v[188:191], v[200:203]\n"
    "v_mfma_f32_16x16x32_bf16 v[200:203], v[88:91], v[212:215], v[200:203]\n"
    "v_mfma_f32_16x16x32_bf16 v[200:203], v[92:95], v[236:239], v[200:203]\n"
    "v_mov_b32 v72, 0x3f803f80\n"
    "v_mov_b32 v73, 0x3f803f80\n"
    "v_mov_b32 v74, 0x3f803f80\n"
    "v_mov_b32 v75, 0x3f803f80\n"
    "v_mfma_f32_16x16x32_bf16 v[224:227], v[108:111], v[168:171], 0\n"
    "v_mfma_f32_16x16x32_bf16 v[224:227], v[112:115], v[192:195], v[224:227]\n"
    "v_max3_f32 v76, v172, v173, v174\n"
    "v_max3_f32 v77, v175, v196, v197\n"
    "v_max3_f32 v76, v76, v198, v199\n"
    "v_max3_f32 v77, v77, v220, v221\n"
    "v_mfma_f32_16x16x32_bf16 v[224:227], v[116:119], v[216:219], v[224:227]\n"
    "v_max3_f32 v76, v76, v222, v223\n"
    "v_max3_f32 v77, v77, v244, v245\n"
    "v_max3_f32 v76, v76, v246, v247\n"
    "v_max3_f32 v77, v77, v176, v177\n"
    "v_mfma_f32_16x16x32_bf16 v[224:227], v[120:123], v[240:243], v[224:227]\n"
    "v_max3_f32 v76, v76, v178, v179\n"
    "v_max3_f32 v77, v77, v200, v201\n"
    "v_max3_f32 v76, v76, v202, v203\n"
    "v_max_f32 v76, v76, v77\n"
    "v_mfma_f32_16x16x32_bf16 v[248:251], v[124:127], v[168:171], 0\n"
    "v_mov_b32 v77, v76\n"
    "s_nop 1\n"
    "v_permlane16_swap_b32 v76, v77\n"
    "v_max_f32 v76, v76, v77\n"
    "v_mov_b32 v77, v76\n"
    "v_mfma_f32_16x16x32_bf16 v[248:251], v[128:131], v[192:195], v[248:251]\n"
    "s_nop 0\n"
    "v_permlane32_swap_b32 v76, v77\n"
    "v_max_f32 v76, v76, v77\n"
    "v_mul_f32 v76, s92, v76\n"
    "v_pk_fma_f32 v[172:173], v[172:173], s[92:93], v[76:77] op_sel_hi:[1,0,0] neg_lo:[0,0,1] neg_hi:[0,0,1]\n"
    "v_mfma_f32_16x16x32_bf16 v[248:251], v[132:135], v[216:219], v[248:251]\n"
    "v_pk_fma_f32 v[174:175], v[174:175], s[92:93], v[76:77] op_sel_hi:[1,0,0] neg_lo:[0,0,1] neg_hi:[0,0,1]\n"
    "v_exp_f32 v172, v172\n"
    "v_exp_f32 v173, v173\n"
    "v_pk_fma_f32 v[196:197], v[196:197], s[92:93], v[76:77] op_sel_hi:[1,0,0] neg_lo:[0,0,1] neg_hi:[0,0,1]\n"
    "v_mfma_f32_16x16x32_bf16 v[248:251], v[136:139], v[240:243], v[248:251]\n"
    "v_exp_f32 v174, v174\n"
    "v_exp_f32 v175, v175\n"
    "v_pk_fma_f32 v[198:199], v[198:199], s[92:93], v[76:77] op_sel_hi:[1,0,0] neg_lo:[0,0,1] neg_hi:[0,0,1]\n"
    "v_exp_f32 v196, v196\n"
    "v_mfma_f32_16x16x32_bf16 v[180:183], v[140:143], v[168:171], 0\n"
    "v_exp_f32 v197, v197\n"
    "v_pk_fma_f32 v[220:221], v[220:221], s[92:93], v[76:77] op_sel_hi:[1,0,0] neg_lo:[0,0,1] neg_hi:[0,0,1]\n"
    "v_exp_f32 v198, v198\n"
    "v_exp_f32 v199, v199\n"
    "v_mfma_f32_16x16x32_bf16 v[180:183], v[144:147], v[192:195], v[180:183]\n"
    "v_pk_fma_f32 v[222:223], v[222:223], s[92:93], v[76:77] op_sel_hi:[1,0,0] neg_lo:[0,0,1] neg_hi:[0,0,1]\n"
    "v_exp_f32 v220, v220\n"
    "v_exp_f32 v221, v221\n"
    "v_cvt_pk_bf16_f32 v172, v172, v173\n"
    "v_mfma_f32_16x16x32_bf16 v[180:183], v[148:151], v[216:219], v[180:183]\n"
    "v_cvt_pk_bf16_f32 v173, v174, v175\n"
    "v_cvt_pk_bf16_f32 v174, v196, v197\n"
    "v_cvt_pk_bf16_f32 v175, v198, v199\n"
    "v_pk_fma_f32 v[244:245], v[244:245], s[92:93], v[76:77] op_sel_hi:[1,0,0] neg_lo:[0,0,1] neg_hi:[0,0,1]\n"
    "v_mfma_f32_16x16x32_bf16 v[180:183], v[152:155], v[240:243], v[180:183]\n"
    "v_exp_f32 v222, v222\n"
    "v_exp_f32 v223, v223\n"
    "v_pk_fma_f32 v[246:247], v[246:247], s[92:93], v[76:77] op_sel_hi:[1,0,0] neg_lo:[0,0,1] neg_hi:[0,0,1]\n"
    "v_exp_f32 v244, v244\n"
    "v_mfma_f32_16x16x32_bf16 v[204:207], v[156:159], v[168:171], 0\n"
    "v_exp_f32 v245, v245\n"
    "v_pk_fma_f32 v[176:177], v[176:177], s[92:93], v[76:77] op_sel_hi:[1,0,0] neg_lo:[0,0,1] neg_hi:[0,0,1]\n"
    "v_exp_f32 v246, v246\n"
    "v_exp_f32 v247, v247\n"
    "v_mfma_f32_16x16x32_bf16 v[204:207], v[20:23], v[192:195], v[204:207]\n"
    "v_pk_fma_f32 v[178:179], v[178:179], s[92:93], v[76:77] op_sel_hi:[1,0,0] neg_lo:[0,0,1] neg_hi:[0,0,1]\n"
    "v_exp_f32 v176, v176\n"
    "v_exp_f32 v177, v177\n"
    "v_cvt_pk_bf16_f32 v220, v220, v221\n"
    "v_mfma_f32_16x16x32_bf16 v[204:207], v[24:27], v[216:219], v[204:207]\n"
    "v_cvt_pk_bf16_f32 v221, v222, v223\n"
    "v_cvt_pk_bf16_f32 v222, v244, v245\n"
    "v_cvt_pk_bf16_f32 v223, v246, v247\n"
    "v_pk_fma_f32 v[200:201], v[200:201], s[92:93], v[76:77] op_sel_hi:[1,0,0] neg_lo:[0,0,1] neg_hi:[0,0,1]\n"
    "v_mfma_f32_16x16x32_bf16 v[204:207], v[28:31], v[240:243], v[204:207]\n"
    "v_exp_f32 v178, v178\n"
    "v_exp_f32 v179, v179\n"
    "v_pk_fma_f32 v[202:203], v[202:203], s[92:93], v[76:77] op_sel_hi:[1,0,0] neg_lo:[0,0,1] neg_hi:[0,0,1]\n"
    "v_exp_f32 v200, v200\n"
    "v_mfma_f32_16x16x32_bf16 v[228:231], v[32:35], v[168:171], 0\n"
    "v_exp_f32 v201, v201\n"
    "v_exp_f32 v202, v202\n"
    "v_exp_f32 v203, v203\n"
    "v_cvt_pk_bf16_f32 v176, v176, v177\n"
    "v_mfma_f32_16x16x32_bf16 v[228:231], v[36:39], v[192:195], v[228:231]\n"
    "v_cvt_pk_bf16_f32 v177, v178, v179\n"
    "v_cvt_pk_bf16_f32 v178, v200, v201\n"
    "v_cvt_pk_bf16_f32 v179, v202, v203\n"
    "v_mfma_f32_16x16x32_bf16 v[228:231], v[40:43], v[216:219], v[228:231]\n"
    "v_mfma_f32_16x16x32_bf16 v[228:231], v[44:47], v[240:243], v[228:231]\n"
    "v_mfma_f32_16x16x32_bf16 v[252:255], v[48:51], v[168:171], 0\n"
    "v_mfma_f32_16x16x32_bf16 v[252:255], v[84:87], v[192:195], v[252:255]\n"
    "v_mfma_f32_16x16x32_bf16 v[252:255], v[88:91], v[216:219], v[252:255]\n"
    "v_mfma_f32_16x16x32_bf16 v[252:255], v[92:95], v[240:243], v[252:255]\n"
    "v_lshlrev_b32 v5, 4, v1\n"
    "s_mul_i32 s52, s10, 12288\n"
    "s_add_u32 s53, s52, 3072\n"
    "s_add_u32 s54, s52, 6144\n"
    "s_add_u32 s55, s52, 9216\n"
    "v_max3_f32 v76, v224, v225, v226\n"
    "v_max3_f32 v77, v227, v248, v249\n"
    "v_max3_f32 v76, v76, v250, v251\n"
    "v_max3_f32 v77, v77, v180, v181\n"
    "v_max3_f32 v76, v76, v182, v183\n"
    "v_max3_f32 v77, v77, v204, v205\n"
    "v_max3_f32 v76, v76, v206, v207\n"
    "v_max3_f32 v77, v77, v228, v229\n"
    "v_max3_f32 v76, v76, v230, v231\n"
    "v_max3_f32 v77, v77, v252, v253\n"
    "v_max3_f32 v76, v76, v254, v255\n"
    "v_max_f32 v76, v76, v77\n"
    "v_mov_b32 v77, v76\n"
    "s_nop 1\n"
    "v_permlane16_swap_b32 v76, v77\n"
    "v_max_f32 v76, v76, v77\n"
    "v_mov_b32 v77, v76\n"
    "s_nop 1\n"
    "v_permlane32_swap_b32 v76, v77\n"
    "v_max_f32 v76, v76, v77\n"
    "v_mul_f32 v76, s92, v76\n"
    "v_pk_fma_f32 v[224:225], v[224:225], s[92:93], v[76:77] op_sel_hi:[1,0,0] neg_lo:[0,0,1] neg_hi:[0,0,1]\n"
    "v_pk_fma_f32 v[226:227], v[226:227], s[92:93], v[76:77] op_sel_hi:[1,0,0] neg_lo:[0,0,1] neg_hi:[0,0,1]\n"
    "v_exp_f32 v224, v224\n"
    "v_exp_f32 v225, v225\n"
    "v_pk_fma_f32 v[248:249], v[248:249], s[92:93], v[76:77] op_sel_hi:[1,0,0] neg_lo:[0,0,1] neg_hi:[0,0,1]\n"
    "v_exp_f32 v226, v226\n"
    "v_exp_f32 v227, v227\n"
    "v_pk_fma_f32 v[250:251], v[250:251], s[92:93], v[76:77] op_sel_hi:[1,0,0] neg_lo:[0,0,1] neg_hi:[0,0,1]\n"
    "v_exp_f32 v248, v248\n"
    "v_exp_f32 v249, v249\n"
    "v_pk_fma_f32 v[180:181], v[180:181], s[92:93], v[76:77] op_sel_hi:[1,0,0] neg_lo:[0,0,1] neg_hi:[0,0,1]\n"
    "v_exp_f32 v250, v250\n"
    "v_exp_f32 v251, v251\n"
    "v_pk_fma_f32 v[182:183], v[182:183], s[92:93], v[76:77] op_sel_hi:[1,0,0] neg_lo:[0,0,1] neg_hi:[0,0,1]\n"
    "v_exp_f32 v180, v180\n"
    "v_exp_f32 v181, v181\n"
    "v_cvt_pk_bf16_f32 v224, v224, v225\n"
    "v_cvt_pk_bf16_f32 v225, v226, v227\n"
    "v_cvt_pk_bf16_f32 v226, v248, v249\n"
    "v_cvt_pk_bf16_f32 v227, v250, v251\n"
    "v_pk_fma_f32 v[204:205], v[204:205], s[92:93], v[76:77] op_sel_hi:[1,0,0] neg_lo:[0,0,1] neg_hi:[0,0,1]\n"
    "v_exp_f32 v182, v182\n"
    "v_exp_f32 v183, v183\n"
    "v_pk_fma_f32 v[206:207], v[206:207], s[92:93], v[76:77] op_sel_hi:[1,0,0] neg_lo:[0,0,1] neg_hi:[0,0,1]\n"
    "v_exp_f32 v204, v204\n"
    "v_exp_f32 v205, v205\n"
    "v_pk_fma_f32 v[228:229], v[228:229], s[92:93], v[76:77] op_sel_hi:[1,0,0] neg_lo:[0,0,1] neg_hi:[0,0,1]\n"
    "v_exp_f32 v206, v206\n"
    "v_exp_f32 v207, v207\n"
    "v_pk_fma_f32 v[230:231], v[230:231], s[92:93], v[76:77] op_sel_hi:[1,0,0] neg_lo:[0,0,1] neg_hi:[0,0,1]\n"
    "v_exp_f32 v228, v228\n"
    "s_barrier\n"
    "ds_read_b64_tr_b16 v[160:161], v8 offset:0\n"
    "v_exp_f32 v229, v229\n"
    "ds_read_b64_tr_b16 v[162:163], v8 offset:4096\n"
    "v_cvt_pk_bf16_f32 v180, v180, v181\n"
    "ds_read_b64_tr_b16 v[184:185], v8 offset:8192\n"
    "v_cvt_pk_bf16_f32 v181, v182, v183\n"
    "ds_read_b64_tr_b16 v[186:187], v8 offset:12288\n"
    "v_cvt_pk_bf16_f32 v182, v204, v205\n"
    "ds_read_b64_tr_b16 v[208:209], v8 offset:16384\n"
    "v_cvt_pk_bf16_f32 v183, v206, v207\n"
    "ds_read_b64_tr_b16 v[210:211], v8 offset:20480\n"
    "v_pk_fma_f32 v[252:253], v[252:253], s[92:93], v[76:77] op_sel_hi:[1,0,0] neg_lo:[0,0,1] neg_hi:[0,0,1]\n"
    "ds_read_b64_tr_b16 v[232:233], v9 offset:0\n"
    "v_exp_f32 v230, v230\n"
    "ds_read_b64_tr_b16 v[234:235], v9 offset:4096\n"
    "v_exp_f32 v231, v231\n"
    "ds_read_b64_tr_b16 v[96:97], v9 offset:8192\n"
    "v_pk_fma_f32 v[254:255], v[254:255], s[92:93], v[76:77] op_sel_hi:[1,0,0] neg_lo:[0,0,1] neg_hi:[0,0,1]\n"
    "ds_read_b64_tr_b16 v[98:99], v9 offset:12288\n"
    "v_exp_f32 v252, v252\n"
    "ds_read_b64_tr_b16 v[100:101], v9 offset:16384\n"
    "v_exp_f32 v253, v253\n"
    "ds_read_b64_tr_b16 v[102:103], v9 offset:20480\n"
    "v_exp_f32 v254, v254\n"
    "ds_read_b64_tr_b16 v[104:105], v10 offset:0\n"
    "v_exp_f32 v255, v255\n"
    "ds_read_b64_tr_b16 v[106:107], v10 offset:4096\n"
    "v_cvt_pk_bf16_f32 v228, v228, v229\n"
    "ds_read_b64_tr_b16 v[164:165], v10 offset:8192\n"
    "v_cvt_pk_bf16_f32 v229, v230, v231\n"
    "ds_read_b64_tr_b16 v[166:167], v10 offset:12288\n"
    "v_cvt_pk_bf16_f32 v230, v252, v253\n"
    "ds_read_b64_tr_b16 v[188:189], v10 offset:16384\n"
    "v_cvt_pk_bf16_f32 v231, v254, v255\n"
    "ds_read_b64_tr_b16 v[190:191], v10 offset:20480\n"
    "v_mfma_f32_16x16x32_bf16 v[80:83], v[72:75], v[52:55], 0\n"
    "ds_read_b64_tr_b16 v[212:213], v11 offset:0\n"
    "ds_read_b64_tr_b16 v[214:215], v11 offset:4096\n"
    "v_mfma_f32_16x16x32_bf16 v[80:83], v[72:75], v[60:63], v[80:83]\n"
    "ds_read_b64_tr_b16 v[236:237], v11 offset:8192\n"
    "ds_read_b64_tr_b16 v[238:239], v11 offset:12288\n"
    "v_mfma_f32_16x16x32_bf16 v[80:83], v[72:75], v[68:71], v[80:83]\n"
    "ds_read_b64_tr_b16 v[56:57], v11 offset:16384\n"
    "ds_read_b64_tr_b16 v[58:59], v11 offset:20480\n"
    "s_waitcnt lgkmcnt(15)\n"
    "v_mfma_f32_16x16x32_bf16 v[152:155], v[160:163], v[52:55], 0\n"
    "ds_read_b64_tr_b16 v[64:65], v12 offset:0\n"
    "ds_read_b64_tr_b16 v[66:67], v12 offset:4096\n"
    "v_mfma_f32_16x16x32_bf16 v[152:155], v[184:187], v[60:63], v[152:155]\n"
    "ds_read_b64_tr_b16 v[108:109], v12 offset:8192\n"
    "ds_read_b64_tr_b16 v[110:111], v12 offset:12288\n"
    "v_mfma_f32_16x16x32_bf16 v[152:155], v[208:211], v[68:71], v[152:155]\n"
    "ds_read_b64_tr_b16 v[112:113], v12 offset:16384\n"
    "ds_read_b64_tr_b16 v[114:115], v12 offset:20480\n"
    "v_mfma_f32_16x16x32_bf16 v[156:159], v[232:235], v[52:55], 0\n"
    "ds_read_b64_tr_b16 v[116:117], v13 offset:0\n"
    "ds_read_b64_tr_b16 v[118:119], v13 offset:4096\n"
    "s_waitcnt lgkmcnt(15)\n"
    "v_mfma_f32_16x16x32_bf16 v[156:159], v[96:99], v[60:63], v[156:159]\n"
    "ds_read_b64_tr_b16 v[120:121], v13 offset:8192\n"
    "ds_read_b64_tr_b16 v[122:123], v13 offset:12288\n"
    "v_mfma_f32_16x16x32_bf16 v[156:159], v[100:103], v[68:71], v[156:159]\n"
    "ds_read_b64_tr_b16 v[124:125], v13 offset:16384\n"
    "ds_read_b64_tr_b16 v[126:127], v13 offset:20480\n"
    "v_mfma_f32_16x16x32_bf16 v[20:23], v[104:107], v[52:55], 0\n"
    "ds_read_b64_tr_b16 v[128:129], v14 offset:0\n"
    "ds_read_b64_tr_b16 v[130:131], v14 offset:4096\n"
    "v_mfma_f32_16x16x32_bf16 v[20:23], v[164:167], v[60:63], v[20:23]\n"
    "ds_read_b64_tr_b16 v[132:133], v14 offset:8192\n"
    "ds_read_b64_tr_b16 v[134:135], v14 offset:12288\n"
    "s_waitcnt lgkmcnt(15)\n"
    "v_mfma_f32_16x16x32_bf16 v[20:23], v[188:191], v[68:71], v[20:23]\n"
    "ds_read_b64_tr_b16 v[136:137], v14 offset:16384\n"
    "ds_read_b64_tr_b16 v[138:139], v14 offset:20480\n"
    "v_mfma_f32_16x16x32_bf16 v[24:27], v[212:215], v[52:55], 0\n"
    "ds_read_b64_tr_b16 v[140:141], v15 offset:0\n"
    "ds_read_b64_tr_b16 v[142:143], v15 offset:4096\n"
    "v_mfma_f32_16x16x32_bf16 v[24:27], v[236:239], v[60:63], v[24:27]\n"
    "ds_read_b64_tr_b16 v[144:145], v15 offset:8192\n"
    "ds_read_b64_tr_b16 v[146:147], v15 offset:12288\n"
    "v_mfma_f32_16x16x32_bf16 v[24:27], v[56:59], v[68:71], v[24:27]\n"
    "ds_read_b64_tr_b16 v[148:149], v15 offset:16384\n"
    "ds_read_b64_tr_b16 v[150:151], v15 offset:20480\n"
    "s_waitcnt lgkmcnt(15)\n"
    "v_mfma_f32_16x16x32_bf16 v[28:31], v[64:67], v[52:55], 0\n"
    "v_rcp_f32 v78, v80\n"
    "v_mfma_f32_16x16x32_bf16 v[28:31], v[108:111], v[60:63], v[28:31]\n"
    "v_mfma_f32_16x16x32_bf16 v[28:31], v[112:115], v[68:71], v[28:31]\n"
    "v_mfma_f32_16x16x32_bf16 v[32:35], v[116:119], v[52:55], 0\n"
    "s_waitcnt lgkmcnt(14)\n"
    "v_mfma_f32_16x16x32_bf16 v[32:35], v[120:123], v[60:63], v[32:35]\n"
    "s_waitcnt lgkmcnt(12)\n"
    "v_mfma_f32_16x16x32_bf16 v[32:35], v[124:127], v[68:71], v[32:35]\n"
    "s_waitcnt lgkmcnt(10)\n"
    "v_mfma_f32_16x16x32_bf16 v[36:39], v[128:131], v[52:55], 0\n"
    "s_waitcnt lgkmcnt(8)\n"
    "v_mfma_f32_16x16x32_bf16 v[36:39], v[132:135], v[60:63], v[36:39]\n"
    "s_waitcnt lgkmcnt(6)\n"
    "v_mfma_f32_16x16x32_bf16 v[36:39], v[136:139], v[68:71], v[36:39]\n"
    "s_waitcnt lgkmcnt(4)\n"
    "v_mfma_f32_16x16x32_bf16 v[40:43], v[140:143], v[52:55], 0\n"
    "s_waitcnt lgkmcnt(2)\n"
    "v_mfma_f32_16x16x32_bf16 v[40:43], v[144:147], v[60:63], v[40:43]\n"
    "s_waitcnt lgkmcnt(0)\n"
    "v_mfma_f32_16x16x32_bf16 v[40:43], v[148:151], v[68:71], v[40:43]\n"
    "v_mfma_f32_16x16x32_bf16 v[80:83], v[72:75], v[172:175], 0\n"
    "v_mfma_f32_16x16x32_bf16 v[80:83], v[72:75], v[220:223], v[80:83]\n"
    "v_pk_mul_f32 v[152:153], v[152:153], v[78:79] op_sel_hi:[1,0]\n"
    "v_pk_mul_f32 v[154:155], v[154:155], v[78:79] op_sel_hi:[1,0]\n"
    "v_mfma_f32_16x16x32_bf16 v[80:83], v[72:75], v[176:179], v[80:83]\n"
    "v_pk_mul_f32 v[156:157], v[156:157], v[78:79] op_sel_hi:[1,0]\n"
    "v_pk_mul_f32 v[158:159], v[158:159], v[78:79] op_sel_hi:[1,0]\n"
    "v_mfma_f32_16x16x32_bf16 v[44:47], v[160:163], v[172:175], 0\n"
    "v_cvt_pk_bf16_f32 v152, v152, v153\n"
    "v_cvt_pk_bf16_f32 v153, v154, v155\n"
    "v_mfma_f32_16x16x32_bf16 v[44:47], v[184:187], v[220:223], v[44:47]\n"
    "v_cvt_pk_bf16_f32 v154, v156, v157\n"
    "v_cvt_pk_bf16_f32 v155, v158, v159\n"
    "v_mfma_f32_16x16x32_bf16 v[44:47], v[208:211], v[176:179], v[44:47]\n"
    "buffer_store_dwordx4 v[152:155], v5, s[12:15], s52 offen offset:0 sc1\n"
    "v_pk_mul_f32 v[20:21], v[20:21], v[78:79] op_sel_hi:[1,0]\n"
    "v_mfma_f32_16x16x32_bf16 v[48:51], v[232:235], v[172:175], 0\n"
    "v_pk_mul_f32 v[22:23], v[22:23], v[78:79] op_sel_hi:[1,0]\n"
    "v_pk_mul_f32 v[24:25], v[24:25], v[78:79] op_sel_hi:[1,0]\n"
    "v_mfma_f32_16x16x32_bf16 v[48:51], v[96:99], v[220:223], v[48:51]\n"
    "v_rcp_f32 v4, v80\n"
    "v_pk_mul_f32 v[26:27], v[26:27], v[78:79] op_sel_hi:[1,0]\n"
    "v_mfma_f32_16x16x32_bf16 v[48:51], v[100:103], v[176:179], v[48:51]\n"
    "v_cvt_pk_bf16_f32 v20, v20, v21\n"
    "v_cvt_pk_bf16_f32 v21, v22, v23\n"
    "v_mfma_f32_16x16x32_bf16 v[84:87], v[104:107], v[172:175], 0\n"
    "v_cvt_pk_bf16_f32 v22, v24, v25\n"
    "v_cvt_pk_bf16_f32 v23, v26, v27\n"
    "v_mfma_f32_16x16x32_bf16 v[84:87], v[164:167], v[220:223], v[84:87]\n"
    "buffer_store_dwordx4 v[20:23], v5, s[12:15], s53 offen offset:0 sc1\n"
    "v_pk_mul_f32 v[28:29], v[28:29], v[78:79] op_sel_hi:[1,0]\n"
    "v_mfma_f32_16x16x32_bf16 v[84:87], v[188:191], v[176:179], v[84:87]\n"
    "v_pk_mul_f32 v[30:31], v[30:31], v[78:79] op_sel_hi:[1,0]\n"
    "v_pk_mul_f32 v[32:33], v[32:33], v[78:79] op_sel_hi:[1,0]\n"
    "v_mfma_f32_16x16x32_bf16 v[88:91], v[212:215], v[172:175], 0\n"
    "v_pk_mul_f32 v[34:35], v[34:35], v[78:79] op_sel_hi:[1,0]\n"
    "v_cvt_pk_bf16_f32 v28, v28, v29\n"
    "v_mfma_f32_16x16x32_bf16 v[88:91], v[236:239], v[220:223], v[88:91]\n"
    "v_cvt_pk_bf16_f32 v29, v30, v31\n"
    "v_cvt_pk_bf16_f32 v30, v32, v33\n"
    "v_mfma_f32_16x16x32_bf16 v[88:91], v[56:59], v[176:179], v[88:91]\n"
    "v_cvt_pk_bf16_f32 v31, v34, v35\n"
    "buffer_store_dwordx4 v[28:31], v5, s[12:15], s54 offen offset:0 sc1\n"
    "v_mfma_f32_16x16x32_bf16 v[92:95], v[64:67], v[172:175], 0\n"
    "v_pk_mul_f32 v[36:37], v[36:37], v[78:79] op_sel_hi:[1,0]\n"
    "v_pk_mul_f32 v[38:39], v[38:39], v[78:79] op_sel_hi:[1,0]\n"
    "v_mfma_f32_16x16x32_bf16 v[92:95], v[108:111], v[220:223], v[92:95]\n"
    "v_pk_mul_f32 v[40:41], v[40:41], v[78:79] op_sel_hi:[1,0]\n"
    "v_pk_mul_f32 v[42:43], v[42:43], v[78:79] op_sel_hi:[1,0]\n"
    "v_mfma_f32_16x16x32_bf16 v[92:95], v[112:115], v[176:179], v[92:95]\n"
    "v_cvt_pk_bf16_f32 v36, v36, v37\n"
    "v_cvt_pk_bf16_f32 v37, v38, v39\n"
    "v_mfma_f32_16x16x32_bf16 v[168:171], v[116:119], v[172:175], 0\n"
    "v_cvt_pk_bf16_f32 v38, v40, v41\n"
    "v_cvt_pk_bf16_f32 v39, v42, v43\n"
    "v_mfma_f32_16x16x32_bf16 v[168:171], v[120:123], v[220:223], v[168:171]\n"
    "buffer_store_dwordx4 v[36:39], v5, s[12:15], s55 offen offset:0 sc1\n"
    "v_mfma_f32_16x16x32_bf16 v[168:171], v[124:127], v[176:179], v[168:171]\n"
    "v_mfma_f32_16x16x32_bf16 v[192:195], v[128:131], v[172:175], 0\n"
    "v_mfma_f32_16x16x32_bf16 v[192:195], v[132:135], v[220:223], v[192:195]\n"
    "v_mfma_f32_16x16x32_bf16 v[192:195], v[136:139], v[176:179], v[192:195]\n"
    "v_mfma_f32_16x16x32_bf16 v[216:219], v[140:143], v[172:175], 0\n"
    "v_mfma_f32_16x16x32_bf16 v[216:219], v[144:147], v[220:223], v[216:219]\n"
    "v_mfma_f32_16x16x32_bf16 v[216:219], v[148:151], v[176:179], v[216:219]\n"
    "v_mfma_f32_16x16x32_bf16 v[80:83], v[72:75], v[224:227], 0\n"
    "v_mfma_f32_16x16x32_bf16 v[80:83], v[72:75], v[180:183], v[80:83]\n"
    "v_pk_mul_f32 v[44:45], v[44:45], v[4:5] op_sel_hi:[1,0]\n"
    "v_pk_mul_f32 v[46:47], v[46:47], v[4:5] op_sel_hi:[1,0]\n"
    "v_mfma_f32_16x16x32_bf16 v[80:83], v[72:75], v[228:231], v[80:83]\n"
    "v_pk_mul_f32 v[48:49], v[48:49], v[4:5] op_sel_hi:[1,0]\n"
    "v_pk_mul_f32 v[50:51], v[50:51], v[4:5] op_sel_hi:[1,0]\n"
    "v_mfma_f32_16x16x32_bf16 v[240:243], v[160:163], v[224:227], 0\n"
    "v_cvt_pk_bf16_f32 v44, v44, v45\n"
    "v_cvt_pk_bf16_f32 v45, v46, v47\n"
    "v_mfma_f32_16x16x32_bf16 v[240:243], v[184:187], v[180:183], v[240:243]\n"
    "v_cvt_pk_bf16_f32 v46, v48, v49\n"
    "v_cvt_pk_bf16_f32 v47, v50, v51\n"
    "v_mfma_f32_16x16x32_bf16 v[240:243], v[208:211], v[228:231], v[240:243]\n"
    "buffer_store_dwordx4 v[44:47], v5, s[12:15], s52 offen offset:1024 sc1\n"
    "v_pk_mul_f32 v[84:85], v[84:85], v[4:5] op_sel_hi:[1,0]\n"
    "v_mfma_f32_16x16x32_bf16 v[196:199], v[232:235], v[224:227], 0\n"
    "v_pk_mul_f32 v[86:87], v[86:87], v[4:5] op_sel_hi:[1,0]\n"
    "v_pk_mul_f32 v[88:89], v[88:89], v[4:5] op_sel_hi:[1,0]\n"
    "v_mfma_f32_16x16x32_bf16 v[196:199], v[96:99], v[180:183], v[196:199]\n"
    "v_rcp_f32 v6, v80\n"
    "v_pk_mul_f32 v[90:91], v[90:91], v[4:5] op_sel_hi:[1,0]\n"
    "v_mfma_f32_16x16x32_bf16 v[196:199], v[100:103], v[228:231], v[196:199]\n"
    "v_cvt_pk_bf16_f32 v84, v84, v85\n"
    "v_cvt_pk_bf16_f32 v85, v86, v87\n"
    "v_mfma_f32_16x16x32_bf16 v[244:247], v[104:107], v[224:227], 0\n"
    "v_cvt_pk_bf16_f32 v86, v88, v89\n"
    "v_cvt_pk_bf16_f32 v87, v90, v91\n"
    "v_mfma_f32_16x16x32_bf16 v[244:247], v[164:167], v[180:183], v[244:247]\n"
    "buffer_store_dwordx4 v[84:87], v5, s[12:15], s53 offen offset:1024 sc1\n"
    "v_pk_mul_f32 v[92:93], v[92:93], v[4:5] op_sel_hi:[1,0]\n"
    "v_mfma_f32_16x16x32_bf16 v[244:247], v[188:191], v[228:231], v[244:247]\n"
    "v_pk_mul_f32 v[94:95], v[94:95], v[4:5] op_sel_hi:[1,0]\n"
    "v_pk_mul_f32 v[168:169], v[168:169], v[4:5] op_sel_hi:[1,0]\n"
    "v_mfma_f32_16x16x32_bf16 v[200:203], v[212:215], v[224:227], 0\n"
    "v_pk_mul_f32 v[170:171], v[170:171], v[4:5] op_sel_hi:[1,0]\n"
    "v_cvt_pk_bf16_f32 v92, v92, v93\n"
    "v_mfma_f32_16x16x32_bf16 v[200:203], v[236:239], v[180:183], v[200:203]\n"
    "v_cvt_pk_bf16_f32 v93, v94, v95\n"
    "v_cvt_pk_bf16_f32 v94, v168, v169\n"
    "v_mfma_f32_16x16x32_bf16 v[200:203], v[56:59], v[228:231], v[200:203]\n"
    "v_cvt_pk_bf16_f32 v95, v170, v171\n"
    "buffer_store_dwordx4 v[92:95], v5, s[12:15], s54 offen offset:1024 sc1\n"
    "v_mfma_f32_16x16x32_bf16 v[248:251], v[64:67], v[224:227], 0\n"
    "v_pk_mul_f32 v[192:193], v[192:193], v[4:5] op_sel_hi:[1,0]\n"
    "v_pk_mul_f32 v[194:195], v[194:195], v[4:5] op_sel_hi:[1,0]\n"
    "v_mfma_f32_16x16x32_bf16 v[248:251], v[108:111], v[180:183], v[248:251]\n"
    "v_pk_mul_f32 v[216:217], v[216:217], v[4:5] op_sel_hi:[1,0]\n"
    "v_pk_mul_f32 v[218:219], v[218:219], v[4:5] op_sel_hi:[1,0]\n"
    "v_mfma_f32_16x16x32_bf16 v[248:251], v[112:115], v[228:231], v[248:251]\n"
    "v_cvt_pk_bf16_f32 v192, v192, v193\n"
    "v_cvt_pk_bf16_f32 v193, v194, v195\n"
    "v_mfma_f32_16x16x32_bf16 v[204:207], v[116:119], v[224:227], 0\n"
    "v_cvt_pk_bf16_f32 v194, v216, v217\n"
    "v_cvt_pk_bf16_f32 v195, v218, v219\n"
    "v_mfma_f32_16x16x32_bf16 v[204:207], v[120:123], v[180:183], v[204:207]\n"
    "buffer_store_dwordx4 v[192:195], v5, s[12:15], s55 offen offset:1024 sc1\n"
    "v_mfma_f32_16x16x32_bf16 v[204:207], v[124:127], v[228:231], v[204:207]\n"
    "v_mfma_f32_16x16x32_bf16 v[252:255], v[128:131], v[224:227], 0\n"
    "v_mfma_f32_16x16x32_bf16 v[252:255], v[132:135], v[180:183], v[252:255]\n"
    "v_mfma_f32_16x16x32_bf16 v[252:255], v[136:139], v[228:231], v[252:255]\n"
    "v_mfma_f32_16x16x32_bf16 v[52:55], v[140:143], v[224:227], 0\n"
    "v_mfma_f32_16x16x32_bf16 v[52:55], v[144:147], v[180:183], v[52:55]\n"
    "v_mfma_f32_16x16x32_bf16 v[52:55], v[148:151], v[228:231], v[52:55]\n"
    "v_pk_mul_f32 v[240:241], v[240:241], v[6:7] op_sel_hi:[1,0]\n"
    "v_pk_mul_f32 v[242:243], v[242:243], v[6:7] op_sel_hi:[1,0]\n"
    "v_pk_mul_f32 v[196:197], v[196:197], v[6:7] op_sel_hi:[1,0]\n"
    "v_pk_mul_f32 v[198:199], v[198:199], v[6:7] op_sel_hi:[1,0]\n"
    "v_cvt_pk_bf16_f32 v240, v240, v241\n"
    "v_cvt_pk_bf16_f32 v241, v242, v243\n"
    "v_cvt_pk_bf16_f32 v242, v196, v197\n"
    "v_cvt_pk_bf16_f32 v243, v198, v199\n"
    "buffer_store_dwordx4 v[240:243], v5, s[12:15], s52 offen offset:2048 sc1\n"
    "v_pk_mul_f32 v[244:245], v[244:245], v[6:7] op_sel_hi:[1,0]\n"
    "v_pk_mul_f32 v[246:247], v[246:247], v[6:7] op_sel_hi:[1,0]\n"
    "v_pk_mul_f32 v[200:201], v[200:201], v[6:7] op_sel_hi:[1,0]\n"
    "v_pk_mul_f32 v[202:203], v[202:203], v[6:7] op_sel_hi:[1,0]\n"
    "v_cvt_pk_bf16_f32 v244, v244, v245\n"
    "v_cvt_pk_bf16_f32 v245, v246, v247\n"
    "v_cvt_pk_bf16_f32 v246, v200, v201\n"
    "v_cvt_pk_bf16_f32 v247, v202, v203\n"
    "buffer_store_dwordx4 v[244:247], v5, s[12:15], s53 offen offset:2048 sc1\n"
    "v_pk_mul_f32 v[248:249], v[248:249], v[6:7] op_sel_hi:[1,0]\n"
    "v_pk_mul_f32 v[250:251], v[250:251], v[6:7] op_sel_hi:[1,0]\n"
    "v_pk_mul_f32 v[204:205], v[204:205], v[6:7] op_sel_hi:[1,0]\n"
    "v_pk_mul_f32 v[206:207], v[206:207], v[6:7] op_sel_hi:[1,0]\n"
    "v_cvt_pk_bf16_f32 v248, v248, v249\n"
    "v_cvt_pk_bf16_f32 v249, v250, v251\n"
    "v_cvt_pk_bf16_f32 v250, v204, v205\n"
    "v_cvt_pk_bf16_f32 v251, v206, v207\n"
    "buffer_store_dwordx4 v[248:251], v5, s[12:15], s54 offen offset:2048 sc1\n"
    "v_pk_mul_f32 v[252:253], v[252:253], v[6:7] op_sel_hi:[1,0]\n"
    "v_pk_mul_f32 v[254:255], v[254:255], v[6:7] op_sel_hi:[1,0]\n"
    "v_pk_mul_f32 v[52:53], v[52:53], v[6:7] op_sel_hi:[1,0]\n"
    "v_pk_mul_f32 v[54:55], v[54:55], v[6:7] op_sel_hi:[1,0]\n"
    "v_cvt_pk_bf16_f32 v252, v252, v253\n"
    "v_cvt_pk_bf16_f32 v253, v254, v255\n"
    "v_cvt_pk_bf16_f32 v254, v52, v53\n"
    "v_cvt_pk_bf16_f32 v255, v54, v55\n"
    "buffer_store_dwordx4 v[252:255], v5, s[12:15], s55 offen offset:2048 sc1\n"
    "s_waitcnt lgkmcnt(0)\n"
    "s_barrier\n"
    "v_mov_b32 v7, v5\n"
    "v_lshrrev_b32 v4, 2, v2\n"
    "v_lshl_add_u32 v4, v3, 2, v4\n"
    "v_mul_u32_u24 v4, 160, v4\n"
    "v_and_b32 v5, 3, v1\n"
    "v_lshl_add_u32 v8, v5, 3, v4\n"
    "s_mov_b32 s57, 0\n"
    "s_mov_b32 s58, 20480\n"
    "s_lshl_b32 s50, s2, 2\n"
    "s_add_u32 s50, s50, s4\n"
    "s_lshl_b32 s93, s50, 7\n"
    "s_add_u32 s50, s93, 20447232\n"
    "v_mov_b32 v4, s36\n"
    "v_mov_b32 v5, s37\n"
    "v_mov_b32 v6, s50\n"
    "ds_read_b64_tr_b16 v[104:105], v8 offset:0\n"
    "ds_read_b64_tr_b16 v[106:107], v8 offset:2560\n"
    "ds_read_b64_tr_b16 v[108:109], v8 offset:32\n"
    "ds_read_b64_tr_b16 v[110:111], v8 offset:2592\n"
    "ds_read_b64_tr_b16 v[112:113], v8 offset:64\n"
    "ds_read_b64_tr_b16 v[114:115], v8 offset:2624\n"
    "ds_read_b64_tr_b16 v[116:117], v8 offset:96\n"
    "ds_read_b64_tr_b16 v[118:119], v8 offset:2656\n"
    "ds_read_b64_tr_b16 v[120:121], v8 offset:128\n"
    "ds_read_b64_tr_b16 v[122:123], v8 offset:2688\n"
    "v_min_u32 v12, 1, v1\n"
    "s_add_u32 s50, s7, 1\n"
    "v_add_u32 v12, s50, v12\n"
    "v_and_b32 v12, 7, v12\n"
    "s_andn2_b32 s50, s93, 0x7000\n"
    "s_add_u32 s50, s50, 20447232\n"
    "v_lshlrev_b32 v12, 12, v12\n"
    "v_add_u32 v12, s50, v12\n"
    "s_andn2_b32 s51, s10, 7\n"
    "s_add_u32 s57, s57, 1\n"
    "s_add_u32 s50, s7, s57\n"
    "s_and_b32 s50, s50, 7\n"
    "s_or_b32 s50, s50, s51\n"
    "s_mul_i32 s53, s50, 12288\n"
    "s_cmp_ge_u32 s57, 8\n"
    "s_cselect_b32 s53, 0x40000000, s53\n"
    "s_mov_b32 s54, s53\n"
    "s_mov_b32 s52, 0x40000000\n"
    "s_waitcnt vmcnt(0)\n"
    "s_mov_b64 exec, 1\n"
    "global_store_dwordx2 v6, v[4:5], s[32:33] sc1\n"
    "s_mov_b64 exec, -1\n"
    "s_waitcnt lgkmcnt(0)\n"
    "v_mfma_f32_16x16x32_bf16 v[24:27], v[104:107], v[152:155], 0\n"
    "ds_read_b64_tr_b16 v[124:125], v8 offset:5120\n"
    "v_mfma_f32_16x16x32_bf16 v[48:51], v[108:111], v[152:155], 0\n"
    "s_nop 5\n"
    "ds_read_b64_tr_b16 v[126:127], v8 offset:7680\n"
    "v_mfma_f32_16x16x32_bf16 v[60:63], v[112:115], v[152:155], 0\n"
    "ds_read_b64_tr_b16 v[128:129], v8 offset:5152\n"
    "v_mfma_f32_16x16x32_bf16 v[72:75], v[116:119], v[152:155], 0\n"
    "s_nop 5\n"
    "ds_read_b64_tr_b16 v[130:131], v8 offset:7712\n"
    "v_mfma_f32_16x16x32_bf16 v[88:91], v[120:123], v[152:155], 0\n"
    "ds_read_b64_tr_b16 v[132:133], v8 offset:5184\n"
    "v_mfma_f32_16x16x32_bf16 v[32:35], v[104:107], v[44:47], 0\n"
    "s_nop 5\n"
    "ds_read_b64_tr_b16 v[134:135], v8 offset:7744\n"
    "v_mfma_f32_16x16x32_bf16 v[52:55], v[108:111], v[44:47], 0\n"
    "ds_read_b64_tr_b16 v[136:137], v8 offset:5216\n"
    "v_mfma_f32_16x16x32_bf16 v[64:67], v[112:115], v[44:47], 0\n"
    "s_nop 5\n"
    "ds_read_b64_tr_b16 v[138:139], v8 offset:7776\n"
    "v_mfma_f32_16x16x32_bf16 v[76:79], v[116:119], v[44:47], 0\n"
    "ds_read_b64_tr_b16 v[140:141], v8 offset:5248\n"
    "v_mfma_f32_16x16x32_bf16 v[96:99], v[120:123], v[44:47], 0\n"
    "s_nop 5\n"
    "ds_read_b64_tr_b16 v[142:143], v8 offset:7808\n"
    "v_mfma_f32_16x16x32_bf16 v[40:43], v[104:107], v[240:243], 0\n"
    "v_mfma_f32_16x16x32_bf16 v[56:59], v[108:111], v[240:243], 0\n"
    "s_nop 5\n"
    "v_mfma_f32_16x16x32_bf16 v[68:71], v[112:115], v[240:243], 0\n"
    "v_mfma_f32_16x16x32_bf16 v[80:83], v[116:119], v[240:243], 0\n"
    "s_nop 5\n"
    "v_mfma_f32_16x16x32_bf16 v[100:103], v[120:123], v[240:243], 0\n"
    "s_waitcnt lgkmcnt(0)\n"
    "v_mfma_f32_16x16x32_bf16 v[24:27], v[124:127], v[20:23], v[24:27]\n"
    "s_nop 5\n"
    "ds_read_b64_tr_b16 v[104:105], v8 offset:10240\n"
    "v_mfma_f32_16x16x32_bf16 v[48:51], v[128:131], v[20:23], v[48:51]\n"
    "ds_read_b64_tr_b16 v[106:107], v8 offset:12800\n"
    "v_mfma_f32_16x16x32_bf16 v[60:63], v[132:135], v[20:23], v[60:63]\n"
    "s_nop 5\n"
    "ds_read_b64_tr_b16 v[108:109], v8 offset:10272\n"
    "v_mfma_f32_16x16x32_bf16 v[72:75], v[136:139], v[20:23], v[72:75]\n"
    "ds_read_b64_tr_b16 v[110:111], v8 offset:12832\n"
    "v_mfma_f32_16x16x32_bf16 v[88:91], v[140:143], v[20:23], v[88:91]\n"
    "s_nop 5\n"
    "ds_read_b64_tr_b16 v[112:113], v8 offset:10304\n"
    "v_mfma_f32_16x16x32_bf16 v[32:35], v[124:127], v[84:87], v[32:35]\n"
    "ds_read_b64_tr_b16 v[114:115], v8 offset:12864\n"
    "v_mfma_f32_16x16x32_bf16 v[52:55], v[128:131], v[84:87], v[52:55]\n"
    "s_nop 5\n"
    "ds_read_b64_tr_b16 v[116:117], v8 offset:10336\n"
    "v_mfma_f32_16x16x32_bf16 v[64:67], v[132:135], v[84:87], v[64:67]\n"
    "ds_read_b64_tr_b16 v[118:119], v8 offset:12896\n"
    "v_mfma_f32_16x16x32_bf16 v[76:79], v[136:139], v[84:87], v[76:79]\n"
    "s_nop 5\n"
    "ds_read_b64_tr_b16 v[120:121], v8 offset:10368\n"
    "v_mfma_f32_16x16x32_bf16 v[96:99], v[140:143], v[84:87], v[96:99]\n"
    "ds_read_b64_tr_b16 v[122:123], v8 offset:12928\n"
    "v_mfma_f32_16x16x32_bf16 v[40:43], v[124:127], v[244:247], v[40:43]\n"
    "s_nop 5\n"
    "v_mfma_f32_16x16x32_bf16 v[56:59], v[128:131], v[244:247], v[56:59]\n"
    "v_mfma_f32_16x16x32_bf16 v[68:71], v[132:135], v[244:247], v[68:71]\n"
    "s_nop 5\n"
    "v_mfma_f32_16x16x32_bf16 v[80:83], v[136:139], v[244:247], v[80:83]\n"
    "v_mfma_f32_16x16x32_bf16 v[100:103], v[140:143], v[244:247], v[100:103]\n"
    "s_nop 5\n"
    "s_waitcnt lgkmcnt(0)\n"
    "v_mfma_f32_16x16x32_bf16 v[24:27], v[104:107], v[28:31], v[24:27]\n"
    "ds_read_b64_tr_b16 v[124:125], v8 offset:15360\n"
    "v_mfma_f32_16x16x32_bf16 v[48:51], v[108:111], v[28:31], v[48:51]\n"
    "s_nop 5\n"
    "ds_read_b64_tr_b16 v[126:127], v8 offset:17920\n"
    "v_mfma_f32_16x16x32_bf16 v[60:63], v[112:115], v[28:31], v[60:63]\n"
    "ds_read_b64_tr_b16 v[128:129], v8 offset:15392\n"
    "v_mfma_f32_16x16x32_bf16 v[72:75], v[116:119], v[28:31], v[72:75]\n"
    "s_nop 5\n"
    "ds_read_b64_tr_b16 v[130:131], v8 offset:17952\n"
    "v_mfma_f32_16x16x32_bf16 v[88:91], v[120:123], v[28:31], v[88:91]\n"
    "ds_read_b64_tr_b16 v[132:133], v8 offset:15424\n"
    "v_mfma_f32_16x16x32_bf16 v[32:35], v[104:107], v[92:95], v[32:35]\n"
    "s_nop 5\n"
    "ds_read_b64_tr_b16 v[134:135], v8 offset:17984\n"
    "v_mfma_f32_16x16x32_bf16 v[52:55], v[108:111], v[92:95], v[52:55]\n"
    "ds_read_b64_tr_b16 v[136:137], v8 offset:15456\n"
    "v_mfma_f32_16x16x32_bf16 v[64:67], v[112:115], v[92:95], v[64:67]\n"
    "s_nop 5\n"
    "ds_read_b64_tr_b16 v[138:139], v8 offset:18016\n"
    "v_mfma_f32_16x16x32_bf16 v[76:79], v[116:119], v[92:95], v[76:79]\n"
    "ds_read_b64_tr_b16 v[140:141], v8 offset:15488\n"
    "v_mfma_f32_16x16x32_bf16 v[96:99], v[120:123], v[92:95], v[96:99]\n"
    "s_nop 5\n"
    "ds_read_b64_tr_b16 v[142:143], v8 offset:18048\n"
    "v_mfma_f32_16x16x32_bf16 v[40:43], v[104:107], v[248:251], v[40:43]\n"
    "v_mfma_f32_16x16x32_bf16 v[56:59], v[108:111], v[248:251], v[56:59]\n"
    "s_nop 5\n"
    "v_mfma_f32_16x16x32_bf16 v[68:71], v[112:115], v[248:251], v[68:71]\n"
    "v_mfma_f32_16x16x32_bf16 v[80:83], v[116:119], v[248:251], v[80:83]\n"
    "s_nop 5\n"
    "v_mfma_f32_16x16x32_bf16 v[100:103], v[120:123], v[248:251], v[100:103]\n"
    "s_waitcnt lgkmcnt(0)\n"
    "v_mfma_f32_16x16x32_bf16 v[24:27], v[124:127], v[36:39], v[24:27]\n"
    "s_nop 5\n"
    "v_add_u32 v8, 20480, v8\n"
    "v_mfma_f32_16x16x32_bf16 v[48:51], v[128:131], v[36:39], v[48:51]\n"
    "ds_read_b64_tr_b16 v[104:105], v8 offset:0\n"
    "v_mfma_f32_16x16x32_bf16 v[60:63], v[132:135], v[36:39], v[60:63]\n"
    "s_nop 5\n"
    "ds_read_b64_tr_b16 v[106:107], v8 offset:2560\n"
    "v_mfma_f32_16x16x32_bf16 v[72:75], v[136:139], v[36:39], v[72:75]\n"
    "ds_read_b64_tr_b16 v[108:109], v8 offset:32\n"
    "v_mfma_f32_16x16x32_bf16 v[88:91], v[140:143], v[36:39], v[88:91]\n"
    "s_nop 5\n"
    "ds_read_b64_tr_b16 v[110:111], v8 offset:2592\n"
    "v_mfma_f32_16x16x32_bf16 v[32:35], v[124:127], v[192:195], v[32:35]\n"
    "ds_read_b64_tr_b16 v[112:113], v8 offset:64\n"
    "v_mfma_f32_16x16x32_bf16 v[52:55], v[128:131], v[192:195], v[52:55]\n"
    "s_nop 5\n"
    "ds_read_b64_tr_b16 v[114:115], v8 offset:2624\n"
    "v_mfma_f32_16x16x32_bf16 v[64:67], v[132:135], v[192:195], v[64:67]\n"
    "ds_read_b64_tr_b16 v[116:117], v8 offset:96\n"
    "v_mfma_f32_16x16x32_bf16 v[76:79], v[136:139], v[192:195], v[76:79]\n"
    "s_nop 5\n"
    "ds_read_b64_tr_b16 v[118:119], v8 offset:2656\n"
    "v_mfma_f32_16x16x32_bf16 v[96:99], v[140:143], v[192:195], v[96:99]\n"
    "ds_read_b64_tr_b16 v[120:121], v8 offset:128\n"
    "v_mfma_f32_16x16x32_bf16 v[40:43], v[124:127], v[252:255], v[40:43]\n"
    "s_nop 5\n"
    "ds_read_b64_tr_b16 v[122:123], v8 offset:2688\n"
    "v_mfma_f32_16x16x32_bf16 v[56:59], v[128:131], v[252:255], v[56:59]\n"
    "v_mfma_f32_16x16x32_bf16 v[68:71], v[132:135], v[252:255], v[68:71]\n"
    "s_nop 5\n"
    "v_mfma_f32_16x16x32_bf16 v[80:83], v[136:139], v[252:255], v[80:83]\n"
    "v_mfma_f32_16x16x32_bf16 v[100:103], v[140:143], v[252:255], v[100:103]\n"
    "s_nop 5\n"
    "s_mov_b32 s50, 131072\n"
    ".Lfz_poll0:\n"
    "global_load_dwordx2 v[14:15], v12, s[32:33] sc1\n"
    "s_waitcnt vmcnt(0)\n"
    "v_cmp_ne_u64 vcc, s[36:37], v[14:15]\n"
    "s_cbranch_vccz .Lfz_poll0_done\n"
    "s_sub_u32 s50, s50, 1\n"
    "s_cbranch_scc0 .Lfz_poll0\n"
    ".Lfz_poll0_done:\n"
    ".Lfz_pbulk:\n"
    "buffer_load_dwordx4 v[152:155], v7, s[12:15], s54 offen offset:0 sc1\n"
    "buffer_load_dwordx4 v[44:47], v7, s[12:15], s54 offen offset:1024 sc1\n"
    "buffer_load_dwordx4 v[240:243], v7, s[12:15], s54 offen offset:2048 sc1\n"
    "s_add_u32 s54, s54, 3072\n"
    "buffer_load_dwordx4 v[20:23], v7, s[12:15], s54 offen offset:0 sc1\n"
    "buffer_load_dwordx4 v[84:87], v7, s[12:15], s54 offen offset:1024 sc1\n"
    "buffer_load_dwordx4 v[244:247], v7, s[12:15], s54 offen offset:2048 sc1\n"
    "s_add_u32 s54, s54, 3072\n"
    "buffer_load_dwordx4 v[28:31], v7, s[12:15], s54 offen offset:0 sc1\n"
    "buffer_load_dwordx4 v[92:95], v7, s[12:15], s54 offen offset:1024 sc1\n"
    "buffer_load_dwordx4 v[248:251], v7, s[12:15], s54 offen offset:2048 sc1\n"
    "s_add_u32 s54, s54, 3072\n"
    "buffer_load_dwordx4 v[36:39], v7, s[12:15], s54 offen offset:0 sc1\n"
    "buffer_load_dwordx4 v[192:195], v7, s[12:15], s54 offen offset:1024 sc1\n"
    "buffer_load_dwordx4 v[252:255], v7, s[12:15], s54 offen offset:2048 sc1\n"
    "s_add_u32 s54, s54, 3072\n"
    "s_add_u32 s57, s57, 1\n"
    "s_add_u32 s50, s7, s57\n"
    "s_and_b32 s50, s50, 7\n"
    "s_or_b32 s50, s50, s51\n"
    "s_mul_i32 s53, s50, 12288\n"
    "s_mov_b32 s52, s53\n"
    ".Lfz_ploop:\n"
    "s_waitcnt lgkmcnt(0)\n"
    "s_waitcnt vmcnt(9)\n"
    "v_mfma_f32_16x16x32_bf16 v[24:27], v[104:107], v[152:155], v[24:27]\n"
    "ds_read_b64_tr_b16 v[124:125], v8 offset:5120\n"
    "v_mfma_f32_16x16x32_bf16 v[48:51], v[108:111], v[152:155], v[48:51]\n"
    "ds_read_b64_tr_b16 v[126:127], v8 offset:7680\n"
    "v_mfma_f32_16x16x32_bf16 v[60:63], v[112:115], v[152:155], v[60:63]\n"
    "ds_read_b64_tr_b16 v[128:129], v8 offset:5152\n"
    "v_mfma_f32_16x16x32_bf16 v[72:75], v[116:119], v[152:155], v[72:75]\n"
    "ds_read_b64_tr_b16 v[130:131], v8 offset:7712\n"
    "v_mfma_f32_16x16x32_bf16 v[88:91], v[120:123], v[152:155], v[88:91]\n"
    "buffer_load_dwordx4 v[152:155], v7, s[12:15], s52 offen offset:0 sc1\n"
    "v_mfma_f32_16x16x32_bf16 v[32:35], v[104:107], v[44:47], v[32:35]\n"
    "ds_read_b64_tr_b16 v[132:133], v8 offset:5184\n"
    "v_mfma_f32_16x16x32_bf16 v[52:55], v[108:111], v[44:47], v[52:55]\n"
    "ds_read_b64_tr_b16 v[134:135], v8 offset:7744\n"
    "v_mfma_f32_16x16x32_bf16 v[64:67], v[112:115], v[44:47], v[64:67]\n"
    "ds_read_b64_tr_b16 v[136:137], v8 offset:5216\n"
    "v_mfma_f32_16x16x32_bf16 v[76:79], v[116:119], v[44:47], v[76:79]\n"
    "ds_read_b64_tr_b16 v[138:139], v8 offset:7776\n"
    "v_mfma_f32_16x16x32_bf16 v[96:99], v[120:123], v[44:47], v[96:99]\n"
    "buffer_load_dwordx4 v[44:47], v7, s[12:15], s52 offen offset:1024 sc1\n"
    "v_mfma_f32_16x16x32_bf16 v[40:43], v[104:107], v[240:243], v[40:43]\n"
    "ds_read_b64_tr_b16 v[140:141], v8 offset:5248\n"
    "v_mfma_f32_16x16x32_bf16 v[56:59], v[108:111], v[240:243], v[56:59]\n"
    "ds_read_b64_tr_b16 v[142:143], v8 offset:7808\n"
    "v_mfma_f32_16x16x32_bf16 v[68:71], v[112:115], v[240:243], v[68:71]\n"
    "v_mfma_f32_16x16x32_bf16 v[80:83], v[116:119], v[240:243], v[80:83]\n"
    "v_mfma_f32_16x16x32_bf16 v[100:103], v[120:123], v[240:243], v[100:103]\n"
    "buffer_load_dwordx4 v[240:243], v7, s[12:15], s52 offen offset:2048 sc1\n"
    "s_add_u32 s52, s52, 3072\n"
    "s_waitcnt lgkmcnt(0)\n"
    "s_waitcnt vmcnt(9)\n"
    "v_mfma_f32_16x16x32_bf16 v[24:27], v[124:127], v[20:23], v[24:27]\n"
    "ds_read_b64_tr_b16 v[104:105], v8 offset:10240\n"
    "v_mfma_f32_16x16x32_bf16 v[48:51], v[128:131], v[20:23], v[48:51]\n"
    "ds_read_b64_tr_b16 v[106:107], v8 offset:12800\n"
    "v_mfma_f32_16x16x32_bf16 v[60:63], v[132:135], v[20:23], v[60:63]\n"
    "ds_read_b64_tr_b16 v[108:109], v8 offset:10272\n"
    "v_mfma_f32_16x16x32_bf16 v[72:75], v[136:139], v[20:23], v[72:75]\n"
    "ds_read_b64_tr_b16 v[110:111], v8 offset:12832\n"
    "v_mfma_f32_16x16x32_bf16 v[88:91], v[140:143], v[20:23], v[88:91]\n"
    "buffer_load_dwordx4 v[20:23], v7, s[12:15], s52 offen offset:0 sc1\n"
    "v_mfma_f32_16x16x32_bf16 v[32:35], v[124:127], v[84:87], v[32:35]\n"
    "ds_read_b64_tr_b16 v[112:113], v8 offset:10304\n"
    "v_mfma_f32_16x16x32_bf16 v[52:55], v[128:131], v[84:87], v[52:55]\n"
    "ds_read_b64_tr_b16 v[114:115], v8 offset:12864\n"
    "v_mfma_f32_16x16x32_bf16 v[64:67], v[132:135], v[84:87], v[64:67]\n"
    "ds_read_b64_tr_b16 v[116:117], v8 offset:10336\n"
    "v_mfma_f32_16x16x32_bf16 v[76:79], v[136:139], v[84:87], v[76:79]\n"
    "ds_read_b64_tr_b16 v[118:119], v8 offset:12896\n"
    "v_mfma_f32_16x16x32_bf16 v[96:99], v[140:143], v[84:87], v[96:99]\n"
    "buffer_load_dwordx4 v[84:87], v7, s[12:15], s52 offen offset:1024 sc1\n"
    "v_mfma_f32_16x16x32_bf16 v[40:43], v[124:127], v[244:247], v[40:43]\n"
    "ds_read_b64_tr_b16 v[120:121], v8 offset:10368\n"
    "v_mfma_f32_16x16x32_bf16 v[56:59], v[128:131], v[244:247], v[56:59]\n"
    "ds_read_b64_tr_b16 v[122:123], v8 offset:12928\n"
    "v_mfma_f32_16x16x32_bf16 v[68:71], v[132:135], v[244:247], v[68:71]\n"
    "v_mfma_f32_16x16x32_bf16 v[80:83], v[136:139], v[244:247], v[80:83]\n"
    "v_mfma_f32_16x16x32_bf16 v[100:103], v[140:143], v[244:247], v[100:103]\n"
    "buffer_load_dwordx4 v[244:247], v7, s[12:15], s52 offen offset:2048 sc1\n"
    "s_add_u32 s52, s52, 3072\n"
    "s_waitcnt lgkmcnt(0)\n"
    "s_waitcnt vmcnt(9)\n"
    "v_mfma_f32_16x16x32_bf16 v[24:27], v[104:107], v[28:31], v[24:27]\n"
    "ds_read_b64_tr_b16 v[124:125], v8 offset:15360\n"
    "v_mfma_f32_16x16x32_bf16 v[48:51], v[108:111], v[28:31], v[48:51]\n"
    "ds_read_b64_tr_b16 v[126:127], v8 offset:17920\n"
    "v_mfma_f32_16x16x32_bf16 v[60:63], v[112:115], v[28:31], v[60:63]\n"
    "ds_read_b64_tr_b16 v[128:129], v8 offset:15392\n"
    "v_mfma_f32_16x16x32_bf16 v[72:75], v[116:119], v[28:31], v[72:75]\n"
    "ds_read_b64_tr_b16 v[130:131], v8 offset:17952\n"
    "v_mfma_f32_16x16x32_bf16 v[88:91], v[120:123], v[28:31], v[88:91]\n"
    "buffer_load_dwordx4 v[28:31], v7, s[12:15], s52 offen offset:0 sc1\n"
    "v_mfma_f32_16x16x32_bf16 v[32:35], v[104:107], v[92:95], v[32:35]\n"
    "ds_read_b64_tr_b16 v[132:133], v8 offset:15424\n"
    "v_mfma_f32_16x16x32_bf16 v[52:55], v[108:111], v[92:95], v[52:55]\n"
    "ds_read_b64_tr_b16 v[134:135], v8 offset:17984\n"
    "v_mfma_f32_16x16x32_bf16 v[64:67], v[112:115], v[92:95], v[64:67]\n"
    "ds_read_b64_tr_b16 v[136:137], v8 offset:15456\n"
    "v_mfma_f32_16x16x32_bf16 v[76:79], v[116:119], v[92:95], v[76:79]\n"
    "ds_read_b64_tr_b16 v[138:139], v8 offset:18016\n"
    "v_mfma_f32_16x16x32_bf16 v[96:99], v[120:123], v[92:95], v[96:99]\n"
    "buffer_load_dwordx4 v[92:95], v7, s[12:15], s52 offen offset:1024 sc1\n"
    "v_mfma_f32_16x16x32_bf16 v[40:43], v[104:107], v[248:251], v[40:43]\n"
    "ds_read_b64_tr_b16 v[140:141], v8 offset:15488\n"
    "ds_read_b64_tr_b16 v[142:143], v8 offset:18048\n"
    "v_mfma_f32_16x16x32_bf16 v[56:59], v[108:111], v[248:251], v[56:59]\n"
    "s_add_u32 s57, s57, 1\n"
    "s_add_u32 s50, s7, s57\n"
    "v_mfma_f32_16x16x32_bf16 v[68:71], v[112:115], v[248:251], v[68:71]\n"
    "s_and_b32 s50, s50, 7\n"
    "s_or_b32 s50, s50, s51\n"
    "v_mfma_f32_16x16x32_bf16 v[80:83], v[116:119], v[248:251], v[80:83]\n"
    "s_mul_i32 s53, s50, 12288\n"
    "v_mfma_f32_16x16x32_bf16 v[100:103], v[120:123], v[248:251], v[100:103]\n"
    "buffer_load_dwordx4 v[248:251], v7, s[12:15], s52 offen offset:2048 sc1\n"
    "s_add_u32 s52, s52, 3072\n"
    "s_waitcnt lgkmcnt(0)\n"
    "s_waitcnt vmcnt(9)\n"
    "v_mfma_f32_16x16x32_bf16 v[24:27], v[124:127], v[36:39], v[24:27]\n"
    "v_add_u32 v8, 20480, v8\n"
    "v_mfma_f32_16x16x32_bf16 v[48:51], v[128:131], v[36:39], v[48:51]\n"
    "ds_read_b64_tr_b16 v[104:105], v8 offset:0\n"
    "v_mfma_f32_16x16x32_bf16 v[60:63], v[132:135], v[36:39], v[60:63]\n"
    "ds_read_b64_tr_b16 v[106:107], v8 offset:2560\n"
    "v_mfma_f32_16x16x32_bf16 v[72:75], v[136:139], v[36:39], v[72:75]\n"
    "ds_read_b64_tr_b16 v[108:109], v8 offset:32\n"
    "v_mfma_f32_16x16x32_bf16 v[88:91], v[140:143], v[36:39], v[88:91]\n"
    "buffer_load_dwordx4 v[36:39], v7, s[12:15], s52 offen offset:0 sc1\n"
    "v_mfma_f32_16x16x32_bf16 v[32:35], v[124:127], v[192:195], v[32:35]\n"
    "ds_read_b64_tr_b16 v[110:111], v8 offset:2592\n"
    "v_mfma_f32_16x16x32_bf16 v[52:55], v[128:131], v[192:195], v[52:55]\n"
    "ds_read_b64_tr_b16 v[112:113], v8 offset:64\n"
    "v_mfma_f32_16x16x32_bf16 v[64:67], v[132:135], v[192:195], v[64:67]\n"
    "ds_read_b64_tr_b16 v[114:115], v8 offset:2624\n"
    "v_mfma_f32_16x16x32_bf16 v[76:79], v[136:139], v[192:195], v[76:79]\n"
    "ds_read_b64_tr_b16 v[116:117], v8 offset:96\n"
    "v_mfma_f32_16x16x32_bf16 v[96:99], v[140:143], v[192:195], v[96:99]\n"
    "buffer_load_dwordx4 v[192:195], v7, s[12:15], s52 offen offset:1024 sc1\n"
    "v_mfma_f32_16x16x32_bf16 v[40:43], v[124:127], v[252:255], v[40:43]\n"
    "ds_read_b64_tr_b16 v[118:119], v8 offset:2656\n"
    "v_mfma_f32_16x16x32_bf16 v[56:59], v[128:131], v[252:255], v[56:59]\n"
    "ds_read_b64_tr_b16 v[120:121], v8 offset:128\n"
    "v_mfma_f32_16x16x32_bf16 v[68:71], v[132:135], v[252:255], v[68:71]\n"
    "ds_read_b64_tr_b16 v[122:123], v8 offset:2688\n"
    "v_mfma_f32_16x16x32_bf16 v[80:83], v[136:139], v[252:255], v[80:83]\n"
    "s_cmp_lt_u32 s57, 8\n"
    "v_mfma_f32_16x16x32_bf16 v[100:103], v[140:143], v[252:255], v[100:103]\n"
    "buffer_load_dwordx4 v[252:255], v7, s[12:15], s52 offen offset:2048 sc1\n"
    "s_mov_b32 s52, s53\n"
    "s_cbranch_scc0 .Lfz_plast\n"
    "s_bitcmp1_b32 8, s57\n"
    "s_cbranch_scc0 .Lfz_ploop\n"
    "s_barrier\n"
    "s_branch .Lfz_ploop\n"
    ".Lfz_plast:\n"
    "s_waitcnt lgkmcnt(0)\n"
    "s_waitcnt vmcnt(9)\n"
    "v_mfma_f32_16x16x32_bf16 v[24:27], v[104:107], v[152:155], v[24:27]\n"
    "ds_read_b64_tr_b16 v[124:125], v8 offset:5120\n"
    "ds_read_b64_tr_b16 v[126:127], v8 offset:7680\n"
    "v_mfma_f32_16x16x32_bf16 v[48:51], v[108:111], v[152:155], v[48:51]\n"
    "ds_read_b64_tr_b16 v[128:129], v8 offset:5152\n"
    "ds_read_b64_tr_b16 v[130:131], v8 offset:7712\n"
    "v_mfma_f32_16x16x32_bf16 v[60:63], v[112:115], v[152:155], v[60:63]\n"
    "ds_read_b64_tr_b16 v[132:133], v8 offset:5184\n"
    "ds_read_b64_tr_b16 v[134:135], v8 offset:7744\n"
    "v_mfma_f32_16x16x32_bf16 v[72:75], v[116:119], v[152:155], v[72:75]\n"
    "ds_read_b64_tr_b16 v[136:137], v8 offset:5216\n"
    "ds_read_b64_tr_b16 v[138:139], v8 offset:7776\n"
    "v_mfma_f32_16x16x32_bf16 v[88:91], v[120:123], v[152:155], v[88:91]\n"
    "ds_read_b64_tr_b16 v[140:141], v8 offset:5248\n"
    "ds_read_b64_tr_b16 v[142:143], v8 offset:7808\n"
    "v_mfma_f32_16x16x32_bf16 v[32:35], v[104:107], v[44:47], v[32:35]\n"
    "ds_read_b64_tr_b16 v[200:201], v8 offset:10240\n"
    "ds_read_b64_tr_b16 v[202:203], v8 offset:12800\n"
    "v_mfma_f32_16x16x32_bf16 v[52:55], v[108:111], v[44:47], v[52:55]\n"
    "ds_read_b64_tr_b16 v[204:205], v8 offset:10272\n"
    "ds_read_b64_tr_b16 v[206:207], v8 offset:12832\n"
    "v_mfma_f32_16x16x32_bf16 v[64:67], v[112:115], v[44:47], v[64:67]\n"
    "ds_read_b64_tr_b16 v[208:209], v8 offset:10304\n"
    "ds_read_b64_tr_b16 v[210:211], v8 offset:12864\n"
    "v_mfma_f32_16x16x32_bf16 v[76:79], v[116:119], v[44:47], v[76:79]\n"
    "ds_read_b64_tr_b16 v[212:213], v8 offset:10336\n"
    "ds_read_b64_tr_b16 v[214:215], v8 offset:12896\n"
    "v_mfma_f32_16x16x32_bf16 v[96:99], v[120:123], v[44:47], v[96:99]\n"
    "ds_read_b64_tr_b16 v[216:217], v8 offset:10368\n"
    "ds_read_b64_tr_b16 v[218:219], v8 offset:12928\n"
    "v_mfma_f32_16x16x32_bf16 v[40:43], v[104:107], v[240:243], v[40:43]\n"
    "v_mfma_f32_16x16x32_bf16 v[56:59], v[108:111], v[240:243], v[56:59]\n"
    "v_mfma_f32_16x16x32_bf16 v[68:71], v[112:115], v[240:243], v[68:71]\n"
    "v_mfma_f32_16x16x32_bf16 v[80:83], v[116:119], v[240:243], v[80:83]\n"
    "v_mfma_f32_16x16x32_bf16 v[100:103], v[120:123], v[240:243], v[100:103]\n"
    "s_waitcnt lgkmcnt(0)\n"
    "s_waitcnt vmcnt(5)\n"
    "v_mfma_f32_16x16x32_bf16 v[24:27], v[124:127], v[20:23], v[24:27]\n"
    "ds_read_b64_tr_b16 v[220:221], v8 offset:15360\n"
    "ds_read_b64_tr_b16 v[222:223], v8 offset:17920\n"
    "v_mfma_f32_16x16x32_bf16 v[48:51], v[128:131], v[20:23], v[48:51]\n"
    "ds_read_b64_tr_b16 v[224:225], v8 offset:15392\n"
    "ds_read_b64_tr_b16 v[226:227], v8 offset:17952\n"
    "v_mfma_f32_16x16x32_bf16 v[60:63], v[132:135], v[20:23], v[60:63]\n"
    "ds_read_b64_tr_b16 v[228:229], v8 offset:15424\n"
    "ds_read_b64_tr_b16 v[230:231], v8 offset:17984\n"
    "v_mfma_f32_16x16x32_bf16 v[72:75], v[136:139], v[20:23], v[72:75]\n"
    "ds_read_b64_tr_b16 v[232:233], v8 offset:15456\n"
    "ds_read_b64_tr_b16 v[234:235], v8 offset:18016\n"
    "v_mfma_f32_16x16x32_bf16 v[88:91], v[140:143], v[20:23], v[88:91]\n"
    "ds_read_b64_tr_b16 v[236:237], v8 offset:15488\n"
    "ds_read_b64_tr_b16 v[238:239], v8 offset:18048\n"
    "v_mfma_f32_16x16x32_bf16 v[24:27], v[200:203], v[28:31], v[24:27]\n"
    "v_and_b32 v4, 1, v3\n"
    "v_lshrrev_b32 v5, 1, v3\n"
    "v_mfma_f32_16x16x32_bf16 v[48:51], v[204:207], v[28:31], v[48:51]\n"
    "v_lshl_add_u32 v9, v5, 4, v2\n"
    "v_lshlrev_b32 v9, 4, v9\n"
    "v_mfma_f32_16x16x32_bf16 v[60:63], v[208:211], v[28:31], v[60:63]\n"
    "v_add_u32 v9, 2048, v9\n"
    "v_lshl_or_b32 v9, v4, 30, v9\n"
    "v_mfma_f32_16x16x32_bf16 v[72:75], v[212:215], v[28:31], v[72:75]\n"
    "s_mul_i32 s59, s6, 2560\n"
    "s_add_u32 s53, s59, 0\n"
    "v_mfma_f32_16x16x32_bf16 v[88:91], v[216:219], v[28:31], v[88:91]\n"
    "s_cmp_eq_u32 s91, 0\n"
    "s_cselect_b32 s53, 0x40000000, s53\n"
    "s_waitcnt lgkmcnt(0)\n"
    "s_waitcnt vmcnt(0)\n"
    "v_mfma_f32_16x16x32_bf16 v[24:27], v[220:223], v[36:39], v[24:27]\n"
    "s_add_u32 s54, s59, 10240\n"
    "s_cmp_eq_u32 s91, 1\n"
    "v_mfma_f32_16x16x32_bf16 v[48:51], v[224:227], v[36:39], v[48:51]\n"
    "s_cselect_b32 s54, 0x40000000, s54\n"
    "s_add_u32 s55, s59, 20480\n"
    "v_mfma_f32_16x16x32_bf16 v[60:63], v[228:231], v[36:39], v[60:63]\n"
    "s_cmp_eq_u32 s91, 2\n"
    "s_cselect_b32 s55, 0x40000000, s55\n"
    "v_mfma_f32_16x16x32_bf16 v[72:75], v[232:235], v[36:39], v[72:75]\n"
    "v_mfma_f32_16x16x32_bf16 v[88:91], v[236:239], v[36:39], v[88:91]\n"
    "v_mfma_f32_16x16x32_bf16 v[32:35], v[124:127], v[84:87], v[32:35]\n"
    "v_mfma_f32_16x16x32_bf16 v[52:55], v[128:131], v[84:87], v[52:55]\n"
    "v_permlane16_swap_b32 v24, v48\n"
    "v_permlane16_swap_b32 v25, v49\n"
    "v_permlane16_swap_b32 v26, v50\n"
    "v_mfma_f32_16x16x32_bf16 v[64:67], v[132:135], v[84:87], v[64:67]\n"
    "v_permlane16_swap_b32 v27, v51\n"
    "v_permlane16_swap_b32 v60, v72\n"
    "v_permlane16_swap_b32 v61, v73\n"
    "v_mfma_f32_16x16x32_bf16 v[76:79], v[136:139], v[84:87], v[76:79]\n"
    "v_permlane16_swap_b32 v62, v74\n"
    "v_permlane16_swap_b32 v63, v75\n"
    "v_cvt_pk_bf16_f32 v184, v24, v25\n"
    "v_mfma_f32_16x16x32_bf16 v[96:99], v[140:143], v[84:87], v[96:99]\n"
    "v_cvt_pk_bf16_f32 v185, v26, v27\n"
    "v_cvt_pk_bf16_f32 v186, v48, v49\n"
    "v_cvt_pk_bf16_f32 v187, v50, v51\n"
    "v_mfma_f32_16x16x32_bf16 v[32:35], v[200:203], v[92:95], v[32:35]\n"
    "buffer_store_dwordx4 v[184:187], v7, s[16:19], s53 offen offset:0 sc1\n"
    "v_mov_b32 v144, v88\n"
    "v_mfma_f32_16x16x32_bf16 v[52:55], v[204:207], v[92:95], v[52:55]\n"
    "v_mov_b32 v145, v89\n"
    "v_mov_b32 v146, v90\n"
    "v_mfma_f32_16x16x32_bf16 v[64:67], v[208:211], v[92:95], v[64:67]\n"
    "v_mov_b32 v147, v91\n"
    "v_cvt_pk_bf16_f32 v188, v60, v61\n"
    "v_mfma_f32_16x16x32_bf16 v[76:79], v[212:215], v[92:95], v[76:79]\n"
    "v_cvt_pk_bf16_f32 v189, v62, v63\n"
    "v_cvt_pk_bf16_f32 v190, v72, v73\n"
    "v_mfma_f32_16x16x32_bf16 v[96:99], v[216:219], v[92:95], v[96:99]\n"
    "v_cvt_pk_bf16_f32 v191, v74, v75\n"
    "buffer_store_dwordx4 v[188:191], v7, s[16:19], s53 offen offset:1024 sc1\n"
    "v_mfma_f32_16x16x32_bf16 v[32:35], v[220:223], v[192:195], v[32:35]\n"
    "v_permlane16_swap_b32 v88, v144\n"
    "v_permlane16_swap_b32 v89, v145\n"
    "v_mfma_f32_16x16x32_bf16 v[52:55], v[224:227], v[192:195], v[52:55]\n"
    "v_permlane16_swap_b32 v90, v146\n"
    "v_permlane16_swap_b32 v91, v147\n"
    "v_mfma_f32_16x16x32_bf16 v[64:67], v[228:231], v[192:195], v[64:67]\n"
    "v_cvt_pk_bf16_f32 v196, v88, v89\n"
    "v_cvt_pk_bf16_f32 v197, v90, v91\n"
    "v_mfma_f32_16x16x32_bf16 v[76:79], v[232:235], v[192:195], v[76:79]\n"
    "v_cvt_pk_bf16_f32 v198, v144, v145\n"
    "v_cvt_pk_bf16_f32 v199, v146, v147\n"
    "v_mfma_f32_16x16x32_bf16 v[96:99], v[236:239], v[192:195], v[96:99]\n"
    "buffer_store_dwordx4 v[196:199], v9, s[16:19], s53 offen offset:0 sc1\n"
    "v_mfma_f32_16x16x32_bf16 v[40:43], v[124:127], v[244:247], v[40:43]\n"
    "v_mfma_f32_16x16x32_bf16 v[56:59], v[128:131], v[244:247], v[56:59]\n"
    "v_permlane16_swap_b32 v32, v52\n"
    "v_permlane16_swap_b32 v33, v53\n"
    "v_permlane16_swap_b32 v34, v54\n"
    "v_mfma_f32_16x16x32_bf16 v[68:71], v[132:135], v[244:247], v[68:71]\n"
    "v_permlane16_swap_b32 v35, v55\n"
    "v_permlane16_swap_b32 v64, v76\n"
    "v_permlane16_swap_b32 v65, v77\n"
    "v_mfma_f32_16x16x32_bf16 v[80:83], v[136:139], v[244:247], v[80:83]\n"
    "v_permlane16_swap_b32 v66, v78\n"
    "v_permlane16_swap_b32 v67, v79\n"
    "v_cvt_pk_bf16_f32 v184, v32, v33\n"
    "v_mfma_f32_16x16x32_bf16 v[100:103], v[140:143], v[244:247], v[100:103]\n"
    "v_cvt_pk_bf16_f32 v185, v34, v35\n"
    "v_cvt_pk_bf16_f32 v186, v52, v53\n"
    "v_cvt_pk_bf16_f32 v187, v54, v55\n"
    "v_mfma_f32_16x16x32_bf16 v[40:43], v[200:203], v[248:251], v[40:43]\n"
    "buffer_store_dwordx4 v[184:187], v7, s[16:19], s54 offen offset:0 sc1\n"
    "v_mov_b32 v148, v96\n"
    "v_mfma_f32_16x16x32_bf16 v[56:59], v[204:207], v[248:251], v[56:59]\n"
    "v_mov_b32 v149, v97\n"
    "v_mov_b32 v150, v98\n"
    "v_mfma_f32_16x16x32_bf16 v[68:71], v[208:211], v[248:251], v[68:71]\n"
    "v_mov_b32 v151, v99\n"
    "v_cvt_pk_bf16_f32 v188, v64, v65\n"
    "v_mfma_f32_16x16x32_bf16 v[80:83], v[212:215], v[248:251], v[80:83]\n"
    "v_cvt_pk_bf16_f32 v189, v66, v67\n"
    "v_cvt_pk_bf16_f32 v190, v76, v77\n"
    "v_mfma_f32_16x16x32_bf16 v[100:103], v[216:219], v[248:251], v[100:103]\n"
    "v_cvt_pk_bf16_f32 v191, v78, v79\n"
    "buffer_store_dwordx4 v[188:191], v7, s[16:19], s54 offen offset:1024 sc1\n"
    "v_mfma_f32_16x16x32_bf16 v[40:43], v[220:223], v[252:255], v[40:43]\n"
    "v_permlane16_swap_b32 v96, v148\n"
    "v_permlane16_swap_b32 v97, v149\n"
    "v_mfma_f32_16x16x32_bf16 v[56:59], v[224:227], v[252:255], v[56:59]\n"
    "v_permlane16_swap_b32 v98, v150\n"
    "v_permlane16_swap_b32 v99, v151\n"
    "v_mfma_f32_16x16x32_bf16 v[68:71], v[228:231], v[252:255], v[68:71]\n"
    "v_cvt_pk_bf16_f32 v196, v96, v97\n"
    "v_cvt_pk_bf16_f32 v197, v98, v99\n"
    "v_mfma_f32_16x16x32_bf16 v[80:83], v[232:235], v[252:255], v[80:83]\n"
    "v_cvt_pk_bf16_f32 v198, v148, v149\n"
    "v_cvt_pk_bf16_f32 v199, v150, v151\n"
    "v_mfma_f32_16x16x32_bf16 v[100:103], v[236:239], v[252:255], v[100:103]\n"
    "buffer_store_dwordx4 v[196:199], v9, s[16:19], s54 offen offset:0 sc1\n"
    "s_cmp_eq_u32 s91, 2\n"
    "s_cbranch_scc1 .Lfz_s2\n"
    "s_nop 3\n"
    "v_permlane16_swap_b32 v40, v56\n"
    "v_permlane16_swap_b32 v41, v57\n"
    "v_permlane16_swap_b32 v42, v58\n"
    "v_permlane16_swap_b32 v43, v59\n"
    "v_permlane16_swap_b32 v68, v80\n"
    "v_permlane16_swap_b32 v69, v81\n"
    "v_permlane16_swap_b32 v70, v82\n"
    "v_permlane16_swap_b32 v71, v83\n"
    "v_cvt_pk_bf16_f32 v184, v40, v41\n"
    "v_cvt_pk_bf16_f32 v185, v42, v43\n"
    "v_cvt_pk_bf16_f32 v186, v56, v57\n"
    "v_cvt_pk_bf16_f32 v187, v58, v59\n"
    "buffer_store_dwordx4 v[184:187], v7, s[16:19], s55 offen offset:0 sc1\n"
    "v_mov_b32 v156, v100\n"
    "v_mov_b32 v157, v101\n"
    "v_mov_b32 v158, v102\n"
    "v_mov_b32 v159, v103\n"
    "v_cvt_pk_bf16_f32 v188, v68, v69\n"
    "v_cvt_pk_bf16_f32 v189, v70, v71\n"
    "v_cvt_pk_bf16_f32 v190, v80, v81\n"
    "v_cvt_pk_bf16_f32 v191, v82, v83\n"
    "buffer_store_dwordx4 v[188:191], v7, s[16:19], s55 offen offset:1024 sc1\n"
    "v_permlane16_swap_b32 v100, v156\n"
    "v_permlane16_swap_b32 v101, v157\n"
    "v_permlane16_swap_b32 v102, v158\n"
    "v_permlane16_swap_b32 v103, v159\n"
    "v_cvt_pk_bf16_f32 v196, v100, v101\n"
    "v_cvt_pk_bf16_f32 v197, v102, v103\n"
    "v_cvt_pk_bf16_f32 v198, v156, v157\n"
    "v_cvt_pk_bf16_f32 v199, v158, v159\n"
    "buffer_store_dwordx4 v[196:199], v9, s[16:19], s55 offen offset:0 sc1\n"
    "s_branch .Lfz_s3\n"
    ".Lfz_s2:\n"
    "s_nop 7\n"
    "v_mov_b32 v156, v100\n"
    "v_mov_b32 v157, v101\n"
    "v_mov_b32 v158, v102\n"
    "v_mov_b32 v159, v103\n"
    "v_permlane16_swap_b32 v40, v56\n"
    "v_permlane16_swap_b32 v41, v57\n"
    "v_permlane16_swap_b32 v42, v58\n"
    "v_permlane16_swap_b32 v43, v59\n"
    "v_permlane16_swap_b32 v68, v80\n"
    "v_permlane16_swap_b32 v69, v81\n"
    "v_permlane16_swap_b32 v70, v82\n"
    "v_permlane16_swap_b32 v71, v83\n"
    "v_permlane16_swap_b32 v100, v156\n"
    "v_permlane16_swap_b32 v101, v157\n"
    "v_permlane16_swap_b32 v102, v158\n"
    "v_permlane16_swap_b32 v103, v159\n"
    ".Lfz_s3:\n"
    "s_cmp_lg_u32 s91, 0\n"
    "s_cbranch_scc1 .Lfz_mv0\n"
    "v_mov_b32 v160, v24\n"
    "v_mov_b32 v161, v25\n"
    "v_mov_b32 v162, v26\n"
    "v_mov_b32 v163, v27\n"
    "v_mov_b32 v164, v48\n"
    "v_mov_b32 v165, v49\n"
    "v_mov_b32 v166, v50\n"
    "v_mov_b32 v167, v51\n"
    "v_mov_b32 v168, v60\n"
    "v_mov_b32 v169, v61\n"
    "v_mov_b32 v170, v62\n"
    "v_mov_b32 v171, v63\n"
    "v_mov_b32 v172, v72\n"
    "v_mov_b32 v173, v73\n"
    "v_mov_b32 v174, v74\n"
    "v_mov_b32 v175, v75\n"
    "v_mov_b32 v176, v88\n"
    "v_mov_b32 v177, v89\n"
    "v_mov_b32 v178, v90\n"
    "v_mov_b32 v179, v91\n"
    "v_mov_b32 v180, v144\n"
    "v_mov_b32 v181, v145\n"
    "v_mov_b32 v182, v146\n"
    "v_mov_b32 v183, v147\n"
    ".Lfz_mv0:\n"
    "s_cmp_lg_u32 s91, 1\n"
    "s_cbranch_scc1 .Lfz_mv1\n"
    "v_mov_b32 v160, v32\n"
    "v_mov_b32 v161, v33\n"
    "v_mov_b32 v162, v34\n"
    "v_mov_b32 v163, v35\n"
    "v_mov_b32 v164, v52\n"
    "v_mov_b32 v165, v53\n"
    "v_mov_b32 v166, v54\n"
    "v_mov_b32 v167, v55\n"
    "v_mov_b32 v168, v64\n"
    "v_mov_b32 v169, v65\n"
    "v_mov_b32 v170, v66\n"
    "v_mov_b32 v171, v67\n"
    "v_mov_b32 v172, v76\n"
    "v_mov_b32 v173, v77\n"
    "v_mov_b32 v174, v78\n"
    "v_mov_b32 v175, v79\n"
    "v_mov_b32 v176, v96\n"
    "v_mov_b32 v177, v97\n"
    "v_mov_b32 v178, v98\n"
    "v_mov_b32 v179, v99\n"
    "v_mov_b32 v180, v148\n"
    "v_mov_b32 v181, v149\n"
    "v_mov_b32 v182, v150\n"
    "v_mov_b32 v183, v151\n"
    ".Lfz_mv1:\n"
    "s_cmp_lg_u32 s91, 2\n"
    "s_cbranch_scc1 .Lfz_mv2\n"
    "v_mov_b32 v160, v40\n"
    "v_mov_b32 v161, v41\n"
    "v_mov_b32 v162, v42\n"
    "v_mov_b32 v163, v43\n"
    "v_mov_b32 v164, v56\n"
    "v_mov_b32 v165, v57\n"
    "v_mov_b32 v166, v58\n"
    "v_mov_b32 v167, v59\n"
    "v_mov_b32 v168, v68\n"
    "v_mov_b32 v169, v69\n"
    "v_mov_b32 v170, v70\n"
    "v_mov_b32 v171, v71\n"
    "v_mov_b32 v172, v80\n"
    "v_mov_b32 v173, v81\n"
    "v_mov_b32 v174, v82\n"
    "v_mov_b32 v175, v83\n"
    "v_mov_b32 v176, v100\n"
    "v_mov_b32 v177, v101\n"
    "v_mov_b32 v178, v102\n"
    "v_mov_b32 v179, v103\n"
    "v_mov_b32 v180, v156\n"
    "v_mov_b32 v181, v157\n"
    "v_mov_b32 v182, v158\n"
    "v_mov_b32 v183, v159\n"
    ".Lfz_mv2:\n"
    "v_mul_u32_u24 v10, 2560, v2\n"
    "v_lshl_add_u32 v10, v5, 4, v10\n"
    "v_add_u32 v11, 128, v10\n"
    "v_lshl_or_b32 v11, v4, 30, v11\n"
    "v_lshl_add_u32 v10, v4, 5, v10\n"
    "s_add_u32 s50, s93, 20578304\n"
    "v_mov_b32 v4, s38\n"
    "v_mov_b32 v5, s39\n"
    "v_mov_b32 v6, s50\n"
    "v_min_u32 v12, 2, v1\n"
    "s_add_u32 s50, s6, 1\n"
    "v_add_u32 v12, s50, v12\n"
    "v_and_b32 v12, 3, v12\n"
    "s_andn2_b32 s50, s93, 0x600\n"
    "s_add_u32 s50, s50, 20578304\n"
    "v_lshlrev_b32 v12, 9, v12\n"
    "v_add_u32 v12, s50, v12\n"
    "s_mul_i32 s52, s91, 10240\n"
    "s_mov_b32 s54, 0x3f80\n"
    "s_mov_b32 s55, 0x3f800000\n"
    "s_add_u32 s50, s6, 1\n"
    "s_and_b32 s50, s50, 3\n"
    "s_mul_i32 s50, s50, 2560\n"
    "s_add_u32 s56, s50, s52\n"
    "s_add_u32 s50, s6, 2\n"
    "s_and_b32 s50, s50, 3\n"
    "s_mul_i32 s50, s50, 2560\n"
    "s_add_u32 s57, s50, s52\n"
    "s_add_u32 s50, s6, 3\n"
    "s_and_b32 s50, s50, 3\n"
    "s_mul_i32 s50, s50, 2560\n"
    "s_add_u32 s58, s50, s52\n"
    "s_waitcnt vmcnt(0)\n"
    "s_mov_b64 exec, 1\n"
    "global_store_dwordx2 v6, v[4:5], s[32:33] sc1\n"
    "s_mov_b64 exec, -1\n"
    "s_cmp_eq_u32 s91, 3\n"
    "s_cbranch_scc1 .Lfz_end\n"
    "s_mov_b32 s50, 131072\n"
    ".Lfz_poll2:\n"
    "global_load_dwordx2 v[14:15], v12, s[32:33] sc1\n"
    "s_waitcnt vmcnt(0)\n"
    "v_cmp_ne_u64 vcc, s[38:39], v[14:15]\n"
    "s_cbranch_vccz .Lfz_poll2_done\n"
    "s_sub_u32 s50, s50, 1\n"
    "s_cbranch_scc0 .Lfz_poll2\n"
    ".Lfz_poll2_done:\n"
    "buffer_load_dwordx4 v[200:203], v7, s[16:19], s56 offen offset:0 sc1\n"
    "buffer_load_dwordx4 v[212:215], v7, s[16:19], s57 offen offset:0 sc1\n"
    "buffer_load_dwordx4 v[224:227], v7, s[16:19], s58 offen offset:0 sc1\n"
    "buffer_load_dwordx4 v[204:207], v7, s[16:19], s56 offen offset:1024 sc1\n"
    "buffer_load_dwordx4 v[216:219], v7, s[16:19], s57 offen offset:1024 sc1\n"
    "buffer_load_dwordx4 v[228:231], v7, s[16:19], s58 offen offset:1024 sc1\n"
    "buffer_load_dwordx4 v[208:211], v9, s[16:19], s56 offen offset:0 sc1\n"
    "buffer_load_dwordx4 v[220:223], v9, s[16:19], s57 offen offset:0 sc1\n"
    "buffer_load_dwordx4 v[232:235], v9, s[16:19], s58 offen offset:0 sc1\n"
    "s_waitcnt vmcnt(8)\n"
    "v_dot2c_f32_bf16 v160, s54, v200\n"
    "v_dot2c_f32_bf16 v161, s55, v200\n"
    "v_dot2c_f32_bf16 v162, s54, v201\n"
    "v_dot2c_f32_bf16 v163, s55, v201\n"
    "v_dot2c_f32_bf16 v164, s54, v202\n"
    "v_dot2c_f32_bf16 v165, s55, v202\n"
    "v_dot2c_f32_bf16 v166, s54, v203\n"
    "v_dot2c_f32_bf16 v167, s55, v203\n"
    "s_waitcnt vmcnt(7)\n"
    "v_dot2c_f32_bf16 v160, s54, v212\n"
    "v_dot2c_f32_bf16 v161, s55, v212\n"
    "v_dot2c_f32_bf16 v162, s54, v213\n"
    "v_dot2c_f32_bf16 v163, s55, v213\n"
    "v_dot2c_f32_bf16 v164, s54, v214\n"
    "v_dot2c_f32_bf16 v165, s55, v214\n"
    "v_dot2c_f32_bf16 v166, s54, v215\n"
    "v_dot2c_f32_bf16 v167, s55, v215\n"
    "s_waitcnt vmcnt(6)\n"
    "v_dot2c_f32_bf16 v160, s54, v224\n"
    "v_dot2c_f32_bf16 v161, s55, v224\n"
    "v_dot2c_f32_bf16 v162, s54, v225\n"
    "v_dot2c_f32_bf16 v163, s55, v225\n"
    "v_dot2c_f32_bf16 v164, s54, v226\n"
    "v_dot2c_f32_bf16 v165, s55, v226\n"
    "v_dot2c_f32_bf16 v166, s54, v227\n"
    "v_dot2c_f32_bf16 v167, s55, v227\n"
    "v_cvt_pk_bf16_f32 v160, v160, v161\n"
    "v_cvt_pk_bf16_f32 v161, v162, v163\n"
    "v_cvt_pk_bf16_f32 v162, v164, v165\n"
    "v_cvt_pk_bf16_f32 v163, v166, v167\n"
    "buffer_store_dwordx4 v[160:163], v10, s[20:23], 0 offen offset:0\n"
    "s_waitcnt vmcnt(6)\n"
    "v_dot2c_f32_bf16 v168, s54, v204\n"
    "v_dot2c_f32_bf16 v169, s55, v204\n"
    "v_dot2c_f32_bf16 v170, s54, v205\n"
    "v_dot2c_f32_bf16 v171, s55, v205\n"
    "v_dot2c_f32_bf16 v172, s54, v206\n"
    "v_dot2c_f32_bf16 v173, s55, v206\n"
    "v_dot2c_f32_bf16 v174, s54, v207\n"
    "v_dot2c_f32_bf16 v175, s55, v207\n"
    "s_waitcnt vmcnt(5)\n"
    "v_dot2c_f32_bf16 v168, s54, v216\n"
    "v_dot2c_f32_bf16 v169, s55, v216\n"
    "v_dot2c_f32_bf16 v170, s54, v217\n"
    "v_dot2c_f32_bf16 v171, s55, v217\n"
    "v_dot2c_f32_bf16 v172, s54, v218\n"
    "v_dot2c_f32_bf16 v173, s55, v218\n"
    "v_dot2c_f32_bf16 v174, s54, v219\n"
    "v_dot2c_f32_bf16 v175, s55, v219\n"
    "s_waitcnt vmcnt(4)\n"
    "v_dot2c_f32_bf16 v168, s54, v228\n"
    "v_dot2c_f32_bf16 v169, s55, v228\n"
    "v_dot2c_f32_bf16 v170, s54, v229\n"
    "v_dot2c_f32_bf16 v171, s55, v229\n"
    "v_dot2c_f32_bf16 v172, s54, v230\n"
    "v_dot2c_f32_bf16 v173, s55, v230\n"
    "v_dot2c_f32_bf16 v174, s54, v231\n"
    "v_dot2c_f32_bf16 v175, s55, v231\n"
    "v_cvt_pk_bf16_f32 v168, v168, v169\n"
    "v_cvt_pk_bf16_f32 v169, v170, v171\n"
    "v_cvt_pk_bf16_f32 v170, v172, v173\n"
    "v_cvt_pk_bf16_f32 v171, v174, v175\n"
    "buffer_store_dwordx4 v[168:171], v10, s[20:23], 0 offen offset:64\n"
    "s_waitcnt vmcnt(4)\n"
    "v_dot2c_f32_bf16 v176, s54, v208\n"
    "v_dot2c_f32_bf16 v177, s55, v208\n"
    "v_dot2c_f32_bf16 v178, s54, v209\n"
    "v_dot2c_f32_bf16 v179, s55, v209\n"
    "v_dot2c_f32_bf16 v180, s54, v210\n"
    "v_dot2c_f32_bf16 v181, s55, v210\n"
    "v_dot2c_f32_bf16 v182, s54, v211\n"
    "v_dot2c_f32_bf16 v183, s55, v211\n"
    "s_waitcnt vmcnt(3)\n"
    "v_dot2c_f32_bf16 v176, s54, v220\n"
    "v_dot2c_f32_bf16 v177, s55, v220\n"
    "v_dot2c_f32_bf16 v178, s54, v221\n"
    "v_dot2c_f32_bf16 v179, s55, v221\n"
    "v_dot2c_f32_bf16 v180, s54, v222\n"
    "v_dot2c_f32_bf16 v181, s55, v222\n"
    "v_dot2c_f32_bf16 v182, s54, v223\n"
    "v_dot2c_f32_bf16 v183, s55, v223\n"
    "s_waitcnt vmcnt(2)\n"
    "v_dot2c_f32_bf16 v176, s54, v232\n"
    "v_dot2c_f32_bf16 v177, s55, v232\n"
    "v_dot2c_f32_bf16 v178, s54, v233\n"
    "v_dot2c_f32_bf16 v179, s55, v233\n"
    "v_dot2c_f32_bf16 v180, s54, v234\n"
    "v_dot2c_f32_bf16 v181, s55, v234\n"
    "v_dot2c_f32_bf16 v182, s54, v235\n"
    "v_dot2c_f32_bf16 v183, s55, v235\n"
    "v_cvt_pk_bf16_f32 v176, v176, v177\n"
    "v_cvt_pk_bf16_f32 v177, v178, v179\n"
    "v_cvt_pk_bf16_f32 v178, v180, v181\n"
    "v_cvt_pk_bf16_f32 v179, v182, v183\n"
    "buffer_store_dwordx4 v[176:179], v11, s[20:23], 0 offen offset:0\n"
    "s_cmp_lg_u32 s91, 0\n"
    "s_cbranch_scc1 .Lfz_end\n"
    "v_mov_b32 v6, s90\n"
    "s_mov_b64 exec, 1\n"
    "global_store_dwordx2 v6, v[4:5], s[32:33] sc1\n"
    "s_mov_b64 exec, -1\n"
    "s_branch .Lfz_end\n"
    ".Lfz_end:\n"
    :: "s"(__builtin_amdgcn_kernarg_segment_ptr()), "s"(blockIdx.x), "v"(threadIdx.x)
    : "s3", "s4", "s5", "s6", "s7", "s8", "s9", "s10", "s11", "s12", "s13", "s14", "s15", "s16", "s17", "s18", "s19", "s20", "s21", "s22", "s23", "s24", "s25", "s26", "s27", "s28", "s29", "s30", "s31", "s33", "s34", "s35", "s36", "s37", "s38", "s39", "s40", "s41", "s42", "s43", "s44", "s45", "s46", "s47", "s48", "s49", "s50", "s51", "s52", "s53", "s54", "s55", "s56", "s57", "s58", "s59", "s60", "s61", "s62", "s63", "s64", "s65", "s66", "s67", "s68", "s69", "s70", "s71", "s72", "s73", "s74", "s75", "s76", "s77", "s78", "s79", "s80", "s81", "s82", "s83", "s84", "s85", "s86", "s87", "s88", "s89", "s90", "s91", "s92", "s93", "s94", "s95", "s96", "s97", "s98", "s99", "v1", "v2", "v3", "v4", "v5", "v6", "v7", "v8", "v9", "v10", "v11", "v12", "v13", "v14", "v15", "v16", "v17", "v18", "v19", "v20", "v21", "v22", "v23", "v24", "v25", "v26", "v27", "v28", "v29", "v30", "v31", "v32", "v33", "v34", "v35", "v36", "v37", "v38", "v39", "v40", "v41", "v42", "v43", "v44", "v45", "v46", "v47", "v48", "v49", "v50", "v51", "v52", "v53", "v54", "v55", "v56", "v57", "v58", "v59", "v60", "v61", "v62", "v63", "v64", "v65", "v66", "v67", "v68", "v69", "v70", "v71", "v72", "v73", "v74", "v75", "v76", "v77", "v78", "v79", "v80", "v81", "v82", "v83", "v84", "v85", "v86", "v87", "v88", "v89", "v90", "v91", "v92", "v93", "v94", "v95", "v96", "v97", "v98", "v99", "v100", "v101", "v102", "v103", "v104", "v105", "v106", "v107", "v108", "v109", "v110", "v111", "v112", "v113", "v114", "v115", "v116", "v117", "v118", "v119", "v120", "v121", "v122", "v123", "v124", "v125", "v126", "v127", "v128", "v129", "v130", "v131", "v132", "v133", "v134", "v135", "v136", "v137", "v138", "v139", "v140", "v141", "v142", "v143", "v144", "v145", "v146", "v147", "v148", "v149", "v150", "v151", "v152", "v153", "v154", "v155", "v156", "v157", "v158", "v159", "v160", "v161", "v162", "v163", "v164", "v165", "v166", "v167", "v168", "v169", "v170", "v171", "v172", "v173", "v174", "v175", "v176", "v177", "v178", "v179", "v180", "v181", "v182", "v183", "v184", "v185", "v186", "v187", "v188", "v189", "v190", "v191", "v192", "v193", "v194", "v195", "v196", "v197", "v198", "v199", "v200", "v201", "v202", "v203", "v204", "v205", "v206", "v207", "v208", "v209", "v210", "v211", "v212", "v213", "v214", "v215", "v216", "v217", "v218", "v219", "v220", "v221", "v222", "v223", "v224", "v225", "v226", "v227", "v228", "v229", "v230", "v231", "v232", "v233", "v234", "v235", "v236", "v237", "v238", "v239", "v240", "v241", "v242", "v243", "v244", "v245", "v246", "v247", "v248", "v249", "v250", "v251", "v252", "v253", "v254", "v255", "vcc", "scc", "memory");
}

extern "C" void kernel_launch(void* const* d_in, const int* in_sizes, int n_in,
                              void* d_out, int out_size, void* d_ws, size_t ws_size,
                              hipStream_t stream) {
  Args a;
  for (int i = 0; i < 10; ++i) a.in[i] = d_in[i];
  a.ws = (char*)d_ws + ((ws_size - (24u << 20)) & ~(size_t)0x1fffff);
  a.out = d_out;
  (void)hipFuncSetAttribute((const void*)fused, hipFuncAttributeMaxDynamicSharedMemorySize, 163840);
  fused<<<256, 512, 163840, stream>>>(a);
}
```
